```python
import torch

def _softmax(x, axis=-1):
    return torch.softmax(x, dim=axis)
def _gelu(x):
    return torch.nn.functional.gelu(x, approximate="tanh")
def _silu(x):
    return torch.nn.functional.silu(x)
def _sigmoid(x):
    return torch.sigmoid(x)
def _layernorm(x, axis=-1, eps=1e-5):
    m = x.mean(dim=axis, keepdim=True)
    v = x.var(dim=axis, keepdim=True, unbiased=False)
    return (x - m) / torch.sqrt(v + eps)
def _scatter_add(tgt, idx, src, axis=0):
    return tgt.clone().index_add_(axis, idx, src)
def _segment_sum(src, seg, n):
    out = torch.zeros((n,) + src.shape[1:], dtype=src.dtype)
    return out.index_add_(0, seg, src)
def _pad(x, pw):
    flat = tuple(v for pair in reversed(pw) for v in pair)
    return torch.nn.functional.pad(x, flat)

kernel_name = 'parallel_sum__compressed_kv_cla__glu_gated_swiglu__decode'

def setup_inputs():

    _w = (lambda a: (lambda t: t.to(torch.bfloat16) if t.is_floating_point() else t)(torch.as_tensor(a)))
    def _A_setup():
        B, H, Sq, Sk, D, M = 2, 32, 1, 8192, 128, 512
        Dc = 4 * D
        return {
            "x": _w((lambda *s: torch.randn(*s, dtype=torch.bfloat16) * 0.05)(B, Sq, M)),
            "c_kv": _w((lambda *s: torch.randn(*s, dtype=torch.bfloat16) * 0.8)(B, Sk, Dc)),
            "w_q": _w((lambda *s: torch.randn(*s, dtype=torch.bfloat16) * 1.6)(M, H, D) * (M ** -0.5)),
            "w_uk": _w((lambda *s: torch.randn(*s, dtype=torch.bfloat16) * 1.6)(Dc, H, D) * (Dc ** -0.5)),
            "w_uv": _w((lambda *s: torch.randn(*s, dtype=torch.bfloat16) * 0.8)(Dc, H, D) * (Dc ** -0.5)),
            "w_o": _w((lambda *s: torch.randn(*s, dtype=torch.bfloat16) * 0.8)(H, D, M) * ((H * D) ** -0.5)),
        }
    _A_in = _A_setup()

    def _B_setup():
        B, S, D, F = 2, 1, 512, 1376
        return {
            "x": _w((lambda *s: torch.randn(*s, dtype=torch.bfloat16) * 0.05)(B, S, D)),
            "w1": _w((lambda *s: torch.randn(*s, dtype=torch.bfloat16) * 0.2)(D, F) * (2.0 / D) ** 0.5),
            "w3": _w((lambda *s: torch.randn(*s, dtype=torch.bfloat16) * 0.2)(D, F) * (2.0 / D) ** 0.5),
            "w2": _w((lambda *s: torch.randn(*s, dtype=torch.bfloat16) * 0.2)(F, D) * (2.0 / F) ** 0.5),
        }
    _B_in = _B_setup()

    return {
        "A_x": _A_in["x"],
        "A_c_kv": _A_in["c_kv"],
        "A_w_q": _A_in["w_q"],
        "A_w_uk": _A_in["w_uk"],
        "A_w_uv": _A_in["w_uv"],
        "A_w_o": _A_in["w_o"],
        "B_x": _B_in["x"],
        "B_w1": _B_in["w1"],
        "B_w3": _B_in["w3"],
        "B_w2": _B_in["w2"],
    }

def reference(A_x, A_c_kv, A_w_q, A_w_uk, A_w_uv, A_w_o, B_x, B_w1, B_w3, B_w2):
    def _A_ref(x, c_kv, w_q, w_uk, w_uv, w_o):
        B, Sq, M = x.shape[0], x.shape[1], x.shape[2]
        Sk = c_kv.shape[1]
        H, D = w_q.shape[1], w_q.shape[2]
        q = torch.einsum("bqm,mhd->bhqd", x, w_q)
        k = torch.einsum("bkc,chd->bhkd", c_kv, w_uk)
        v = torch.einsum("bkc,chd->bhkd", c_kv, w_uv)
        scores = torch.einsum("bhqd,bhkd->bhqk", q, k) * (D ** -0.5)
        off = Sk - Sq
        qi = torch.arange(Sq)[:, None]
        kj = torch.arange(Sk)[None, :]
        cmask = kj <= (qi + off)
        scores = torch.where(cmask[None, None], scores, -1e9)
        p = _softmax(scores, axis=-1)
        ctx = torch.einsum("bhqk,bhkd->bhqd", p, v)
        return torch.einsum("bhqd,hdm->bqm", ctx, w_o)

    def _B_ref(x, w1, w3, w2):
        gate = torch.einsum("bsd,df->bsf", x, w1)
        up = torch.einsum("bsd,df->bsf", x, w3)
        h = _silu(gate) * up
        out = torch.einsum("bsf,fd->bsd", h, w2)
        return out

    x = A_x
    xn = _layernorm(x, axis=-1)
    return x + _A_ref(xn, A_c_kv, A_w_q, A_w_uk, A_w_uv, A_w_o) + _B_ref(xn, B_w1, B_w3, B_w2)
```

```cpp
#include <hip/hip_runtime.h>
#include <hip/hip_bf16.h>
typedef unsigned short u16;
__device__ __forceinline__ float bf2f(u16 v) { return __uint_as_float(((unsigned)v) << 16); }
__device__ __forceinline__ u16 f2bf(float f) { __hip_bfloat16 h = __float2bfloat16(f); return *(u16*)&h; }
constexpr int B=2,H=32,SK=8192,D=128,M=512,DC=512,F=1376;

__device__ void layernorm_to_lds(const u16* x, float* xn  , float* red) {
    int t = threadIdx.x;
    for (int b = 0; b < 2; ++b) {
        float v0 = bf2f(x[b*M + t]), v1 = bf2f(x[b*M + t + 256]);
        red[t] = v0 + v1; __syncthreads();
        for (int s = 128; s > 0; s >>= 1) { if (t < s) red[t] += red[t+s]; __syncthreads(); }
        float mean = red[0] / M; __syncthreads();
        float d0 = v0 - mean, d1 = v1 - mean;
        red[t] = d0*d0 + d1*d1; __syncthreads();
        for (int s = 128; s > 0; s >>= 1) { if (t < s) red[t] += red[t+s]; __syncthreads(); }
        float var = red[0] / M; __syncthreads();
        float r = 1.f / sqrtf(var + 1e-5f);
        xn[b*M + t] = d0 * r; xn[b*M + t + 256] = d1 * r;
    }
    __syncthreads();
}
__global__ void k_q(const u16* x, const u16* wq, const u16* wuk, float* qabs) {
    __shared__ float xn[2*M]; __shared__ float red[256]; __shared__ float q[2*D];
    int h = blockIdx.x, t = threadIdx.x;
    layernorm_to_lds(x, xn, red);
    { int b = t >> 7, d = t & 127; float a = 0;
      for (int m = 0; m < M; ++m) a += xn[b*M+m] * bf2f(wq[(m*H + h)*D + d]);
      q[b*D + d] = a; }
    __syncthreads();
    for (int i = t; i < 2*DC; i += 256) { int b = i / DC, c = i % DC; float a = 0;
      for (int d = 0; d < D; ++d) a += q[b*D+d] * bf2f(wuk[(c*H + h)*D + d]);
      qabs[(b*H + h)*DC + c] = a * 0.08838834764831845f; }
}
__global__ void k_s(const u16* ckv, const float* qabs, float* s) {
    int b = blockIdx.x, k0 = blockIdx.y * 64, t = threadIdx.x;
    for (int i = t; i < 64*H; i += 256) { int k = k0 + (i & 63), h = i >> 6; float a = 0;
      for (int c = 0; c < DC; ++c) a += qabs[(b*H+h)*DC + c] * bf2f(ckv[((size_t)b*SK + k)*DC + c]);
      s[((size_t)b*H + h)*SK + k] = a; }
}
__global__ void k_pv(const u16* ckv, const float* s, float* ctxc) {
    __shared__ float red[256]; __shared__ float p[SK];
    int bh = blockIdx.x, b = bh / H, t = threadIdx.x;
    float mx = -1e30f; for (int k = t; k < SK; k += 256) mx = fmaxf(mx, s[(size_t)bh*SK + k]);
    red[t] = mx; __syncthreads();
    for (int st = 128; st > 0; st >>= 1) { if (t < st) red[t] = fmaxf(red[t], red[t+st]); __syncthreads(); }
    mx = red[0]; __syncthreads();
    float sum = 0; for (int k = t; k < SK; k += 256) { float e = expf(s[(size_t)bh*SK + k] - mx); p[k] = e; sum += e; }
    red[t] = sum; __syncthreads();
    for (int st = 128; st > 0; st >>= 1) { if (t < st) red[t] += red[t+st]; __syncthreads(); }
    float inv = 1.f / red[0];
    for (int c = t; c < DC; c += 256) { float a = 0;
      for (int k = 0; k < SK; ++k) a += p[k] * bf2f(ckv[((size_t)b*SK + k)*DC + c]);
      ctxc[bh*DC + c] = a * inv; }
}
__global__ void k_v(const float* ctxc, const u16* wuv, float* ctx) {
    int bh = blockIdx.x, h = bh % H, d = threadIdx.x; float a = 0;
    for (int c = 0; c < DC; ++c) a += ctxc[bh*DC + c] * bf2f(wuv[(c*H + h)*D + d]);
    ctx[bh*D + d] = a;
}
__global__ void k_ffn(const u16* x, const u16* w1, const u16* w3, float* hid) {
    __shared__ float xn[2*M]; __shared__ float red[256];
    layernorm_to_lds(x, xn, red);
    int t = threadIdx.x; int f = blockIdx.x * 128 + (t & 127), b = t >> 7;
    if (f < F) { float g = 0, u = 0;
      for (int m = 0; m < M; ++m) { g += xn[b*M+m] * bf2f(w1[m*F + f]); u += xn[b*M+m] * bf2f(w3[m*F + f]); }
      hid[b*F + f] = g / (1.f + expf(-g)) * u; }
}
__global__ void k_out(const u16* x, const float* ctx, const u16* wo, const float* hid, const u16* w2, u16* out) {
    int i = blockIdx.x * 64 + threadIdx.x; int b = i / M, m = i % M; float a = bf2f(x[i]);
    for (int k = 0; k < H*D; ++k) a += ctx[b*H*D + k] * bf2f(wo[(size_t)k*M + m]);
    for (int f = 0; f < F; ++f) a += hid[b*F + f] * bf2f(w2[f*M + m]);
    out[i] = f2bf(a);
}
extern "C" void kernel_launch(void* const* d_in, const int* in_sizes, int n_in,
                              void* d_out, int out_size, void* d_ws, size_t ws_size,
                              hipStream_t stream) {
    const u16* x = (const u16*)d_in[0]; const u16* ckv = (const u16*)d_in[1];
    const u16* wq = (const u16*)d_in[2]; const u16* wuk = (const u16*)d_in[3];
    const u16* wuv = (const u16*)d_in[4]; const u16* wo = (const u16*)d_in[5];
    const u16* w1 = (const u16*)d_in[7]; const u16* w3 = (const u16*)d_in[8]; const u16* w2 = (const u16*)d_in[9];
    float* ws = (float*)d_ws;
    float* qabs = ws; float* s = qabs + B*H*DC; float* ctxc = s + (size_t)B*H*SK; float* ctx = ctxc + B*H*DC; float* hid = ctx + B*H*D;
    k_q<<<H, 256, 0, stream>>>(x, wq, wuk, qabs);
    k_s<<<dim3(B, SK/64), 256, 0, stream>>>(ckv, qabs, s);
    k_pv<<<B*H, 256, 0, stream>>>(ckv, s, ctxc);
    k_v<<<B*H, 128, 0, stream>>>(ctxc, wuv, ctx);
    k_ffn<<<(F+127)/128, 256, 0, stream>>>(x, w1, w3, hid);
    k_out<<<16, 64, 0, stream>>>(x, ctx, wo, hid, w2, (u16*)d_out);
}
```

```cpp
#include <hip/hip_runtime.h>
typedef unsigned u32; typedef unsigned long long u64; typedef unsigned short u16;
typedef u32 u32x4 __attribute__((ext_vector_type(4)));
typedef u32 u32x2 __attribute__((ext_vector_type(2)));
typedef float f32x4 __attribute__((ext_vector_type(4)));
typedef float f32x2 __attribute__((ext_vector_type(2)));
typedef __bf16 bf16x8 __attribute__((ext_vector_type(8)));
typedef short s16x4 __attribute__((ext_vector_type(4)));
#define LDSP(T) __attribute__((address_space(3))) T*
#define NSTAMP 0
constexpr u32 WS_QA = 4096;
constexpr u32 WS_FP = WS_QA + 131072;
constexpr u32 WS_ST = WS_FP + 360448;
constexpr u32 WS_OP = WS_ST + 65536;
constexpr u32 WS_PT = WS_OP + 1048576;
constexpr u32 L_CKV = 0, L_Q = 131072, L_P = 147456, L_XD = 151552, L_CNT = 155648, L_WM = 155776, L_WL = 156032,
              L_QV = 156288  , L_HD = 158336, L_F1 = 158464  , L_END = 162560;
constexpr u32 L_QPART = L_P;
constexpr u32 L_CPART = 0, L_VPART = 2048, L_CD = 6144, L_OPART = 8192, L_FIN = 16384;
constexpr int SPIN_MAX = 20000;

__device__ __forceinline__ f32x4 mfma(u32x4 a, u32x4 b, f32x4 c) {
  return __builtin_amdgcn_mfma_f32_16x16x32_bf16(__builtin_bit_cast(bf16x8, a), __builtin_bit_cast(bf16x8, b), c, 0, 0, 0); }
typedef __bf16 bf16x2 __attribute__((ext_vector_type(2)));
__device__ __forceinline__ u32 pkbf(float a, float b) { return __builtin_bit_cast(u32, __builtin_convertvector(f32x2{a, b}, bf16x2)); }
__device__ __forceinline__ float asf(u32 v) { return __builtin_bit_cast(float, v); }
__device__ __forceinline__ u32 asu(float v) { return __builtin_bit_cast(u32, v); }
#define GLD(dst, voff, sbase) asm volatile("global_load_dwordx4 %0, %1, %2" : "=v"(dst) : "v"(voff), "s"(sbase))
#define GLDC(dst, voff, sbase) asm volatile("global_load_dwordx4 %0, %1, %2 sc1" : "=v"(dst) : "v"(voff), "s"(sbase))
#define GSTC(voff, data, sbase) asm volatile("global_store_dwordx4 %0, %1, %2 sc1\n\ts_nop 2" :: "v"(voff), "v"(data), "s"(sbase) : "memory")
#define GLDS(voff, sbase, ldsaddr) asm volatile("s_mov_b32 m0, %2\n\ts_nop 0\n\tglobal_load_lds_dwordx4 %0, %1" :: "v"(voff), "s"(sbase), "s"(ldsaddr) : "memory")
#define WAITV(n) asm volatile("s_waitcnt vmcnt(%0)" :: "n"(n) : "memory")
#define TIE(r) asm volatile("" : "+v"(r))
#define SB() __builtin_amdgcn_sched_barrier(0)
#define WAIT4(n, a, b, c, d) do { asm volatile("s_waitcnt vmcnt(%4)" : "+v"(a), "+v"(b), "+v"(c), "+v"(d) : "n"(n) : "memory"); } while (0)
#define WAIT2(n, a, b) do { asm volatile("s_waitcnt vmcnt(%2)" : "+v"(a), "+v"(b) : "n"(n) : "memory"); } while (0)
#define WAIT1(n, a) do { asm volatile("s_waitcnt vmcnt(%1)" : "+v"(a) : "n"(n) : "memory"); } while (0)
#define POLL1(a, o0, sb) asm volatile("global_load_dwordx4 %0, %1, %2 sc1\n\ts_waitcnt vmcnt(0)" : "=&v"(a) : "v"(o0), "s"(sb) : "memory")
#define POLL2(a, b, o0, o1, sb) asm volatile("global_load_dwordx4 %0, %2, %4 sc1\n\tglobal_load_dwordx4 %1, %3, %4 sc1\n\ts_waitcnt vmcnt(0)" : "=&v"(a), "=&v"(b) : "v"(o0), "v"(o1), "s"(sb) : "memory")
#define POLL4(a, b, c, d, o0, sb, i1, i2, i3) asm volatile("global_load_dwordx4 %0, %4, %8 sc1\n\tglobal_load_dwordx4 %1, %5, %8 sc1\n\tglobal_load_dwordx4 %2, %6, %8 sc1\n\tglobal_load_dwordx4 %3, %7, %8 sc1\n\ts_waitcnt vmcnt(0)" \
   : "=&v"(a), "=&v"(b), "=&v"(c), "=&v"(d) : "v"(o0), "v"((o0) + (i1)), "v"((o0) + (i2)), "v"((o0) + (i3)), "s"(sb) : "memory")
#define TAGOK(q) (((q)[1] == TL) & ((q)[3] == TH))
#define BAR() asm volatile("s_waitcnt lgkmcnt(0)\n\ts_barrier" ::: "memory")
template <int CTRL> __device__ __forceinline__ float dppf(float v) { return asf(__builtin_amdgcn_update_dpp(0, asu(v), CTRL, 0xf, 0xf, true)); }
__device__ __forceinline__ float sum16x(float v) { auto r = __builtin_amdgcn_permlane16_swap(asu(v), asu(v), false, false); return asf(r[0]) + asf(r[1]); }
__device__ __forceinline__ float sum32x(float v) { auto r = __builtin_amdgcn_permlane32_swap(asu(v), asu(v), false, false); return asf(r[0]) + asf(r[1]); }
__device__ __forceinline__ float max16x(float v) { auto r = __builtin_amdgcn_permlane16_swap(asu(v), asu(v), false, false); return fmaxf(asf(r[0]), asf(r[1])); }
__device__ __forceinline__ float max32x(float v) { auto r = __builtin_amdgcn_permlane32_swap(asu(v), asu(v), false, false); return fmaxf(asf(r[0]), asf(r[1])); }
__device__ __forceinline__ float wsum(float v) { v += dppf<0xB1>(v); v += dppf<0x4E>(v); v += dppf<0x141>(v); v += dppf<0x140>(v); v = sum16x(v); return sum32x(v); }
__device__ __forceinline__ float wmax(float v) { v = fmaxf(v, dppf<0xB1>(v)); v = fmaxf(v, dppf<0x4E>(v)); v = fmaxf(v, dppf<0x141>(v)); v = fmaxf(v, dppf<0x140>(v)); v = max16x(v); return max32x(v); }
__device__ __forceinline__ u32 swz(u32 r) { return ((r & 3) << 2) | (((r >> 3) & 1) << 1); }

#define STAMP(k) do {} while (0)

__global__ __launch_bounds__(1024) void mla_ffn_fused(const char* __restrict__ x, const char* __restrict__ ckv, const char* __restrict__ wq,
    const char* __restrict__ wuk, const char* __restrict__ wuv, const char* __restrict__ wo, const char* __restrict__ w1,
    const char* __restrict__ w3, const char* __restrict__ w2, u16* __restrict__ out, char* __restrict__ ws) {
  __shared__ __attribute__((aligned(1024))) char smem[L_END];
  LDSP(char) sm = (LDSP(char))smem;
#define LD(T, off) (*(LDSP(T))(sm + (off)))
#define LDV(T, off) (*(volatile LDSP(T))(sm + (off)))
  const u32 tid = threadIdx.x, lane = tid & 63;
  const u32 wid = __builtin_amdgcn_readfirstlane(tid >> 6);
  STAMP(0);
  if (tid < 16) LD(u32, L_CNT + 4 * tid) = 0;
  BAR();
  const u32 smb = (u32)(size_t)sm;
  auto signal = [&](u32 k) { asm volatile("" ::: "memory"); if (lane == 0) asm volatile("ds_add_u32 %0, %1" :: "v"(smb + L_CNT + 4 * k), "v"(1u) : "memory"); asm volatile("" ::: "memory"); };
  auto waitc = [&](u32 k, u32 n) { _Pragma("nounroll") for (int it = 0; it < 4 * SPIN_MAX; ++it) { if (__builtin_amdgcn_readfirstlane(LDV(u32, L_CNT + 4 * k)) >= n) break; __builtin_amdgcn_s_sleep(1); } asm volatile("" ::: "memory"); };
  const u32 wg = blockIdx.x, xc = wg & 7, ii = wg >> 3;
  const u32 h = 4 * xc + (ii >> 3), b = ii & 1, j = (ii >> 1) & 3, prod = h * 4 + j;
  const u32 T = xc * 16 + (ii >> 1), ba = T >> 6, kt = T & 63, hh = ii & 1;
  const u32 r = lane & 15, g = lane >> 4;
  u64 E0, E; asm volatile("s_load_dwordx2 %0, %1, 0x0" : "=s"(E0) : "s"(ws));
  u32 TL, TH;
#define GET_TAGS() do { asm volatile("s_waitcnt lgkmcnt(0)\n\ts_mov_b64 %0, %1" : "=s"(E) : "s"(E0)); \
  u64 z = E + 0x9E3779B97F4A7C15ull; z = (z ^ (z >> 30)) * 0xBF58476D1CE4E5B9ull; z = (z ^ (z >> 27)) * 0x94D049BB133111EBull; z ^= z >> 31; \
  TL = (u32)z & 0xBFFFBFFFu; TH = (u32)(z >> 32) & 0xBFFFBFFFu; } while (0)
  u32x4 mk;
  { const u32 hm = (r & 1) ? 0xFFFF0000u : 0x0000FFFFu, t = (r & 7) >> 1; mk = u32x4{t == 0 ? hm : 0u, t == 1 ? hm : 0u, t == 2 ? hm : 0u, t == 3 ? hm : 0u}; }
#define DB(xd) (u32x4{(xd) & mk[0], (xd) & mk[1], (xd) & mk[2], (xd) & mk[3]})

  if (wid < 8) {
    const u32 v = wid;
    __builtin_amdgcn_s_setprio(3);
    u32x4 xr;
    if (v < 2) { GLD(xr, v * 1024 + lane * 16, x); }
    u32x4 wqr[16];
    const u32 wb = (v + ii) & 7;
    { const u32 o = (64 * wb + g) * 8192 + h * 256 + r * 16;
      #pragma unroll
      for (int k = 0; k < 16; ++k) GLD(wqr[k], o + k * 32768, wq); }
    u32x4 ukr[4];
    { const u32 o = (128 * j + 16 * v + r) * 8192 + h * 256 + g * 16;
      #pragma unroll
      for (int k = 0; k < 4; ++k) GLD(ukr[k], o + k * 64, wuk); }
    STAMP(1);
    if (v < 2) {
      WAIT1(20, xr);
      float f[8];
      #pragma unroll
      for (int e = 0; e < 4; ++e) { f[2 * e] = asf(xr[e] << 16); f[2 * e + 1] = asf(xr[e] & 0xFFFF0000u); }
      float s = 0; for (int e = 0; e < 8; ++e) s += f[e];
      const float mean = wsum(s) * (1.f / 512);
      float q = 0; for (int e = 0; e < 8; ++e) { f[e] -= mean; q += f[e] * f[e]; }
      const float rstd = 1.f / sqrtf(wsum(q) * (1.f / 512) + 1e-5f);
      #pragma unroll
      for (int e = 0; e < 8; ++e) { const u32 d = pkbf(f[e] * rstd, f[e] * rstd);
        LD(u32, L_XD + v * 2048 + 4 * ((lane >> 3) * 64 + (e & 3) * 16 + 2 * (lane & 7) + (e >> 2))) = d; }
      signal(0);
    }
    waitc(0, 2);
    GET_TAGS();
    STAMP(2);
    { u32x4 xd[4];
      #pragma unroll
      for (int k = 0; k < 4; ++k) xd[k] = LD(u32x4, L_XD + b * 2048 + 4 * (wb * 64 + g * 16 + 4 * k));
      f32x4 a0 = {0, 0, 0, 0}, a1 = {0, 0, 0, 0};
      #pragma unroll
      for (int k = 0; k < 4; ++k) {
        SB(); WAIT4(16 - 4 * k, wqr[4 * k], wqr[4 * k + 1], wqr[4 * k + 2], wqr[4 * k + 3]);
        a0 = mfma(wqr[4 * k], DB(xd[k][0]), a0); a1 = mfma(wqr[4 * k + 1], DB(xd[k][1]), a1);
        a0 = mfma(wqr[4 * k + 2], DB(xd[k][2]), a0); a1 = mfma(wqr[4 * k + 3], DB(xd[k][3]), a1);
      }
      a0 += a1;
      const u32 d0 = 32 * g + 16 * (r >> 3) + (r & 7);
      LD(float, L_QPART + v * 512 + 4 * d0) = (r < 8) ? a0[0] : a0[2];
      LD(float, L_QPART + v * 512 + 4 * d0 + 32) = (r < 8) ? a0[1] : a0[3];
    }
    signal(1); waitc(1, 8);
    STAMP(3);
    { f32x2 s = LD(f32x2, L_QPART + 8 * lane);
      #pragma unroll
      for (int k = 1; k < 8; ++k) s += LD(f32x2, L_QPART + k * 512 + 8 * lane);
      LD(u32, L_QV + v * 256 + 4 * lane) = pkbf(s[0], s[1]);
      WAIT4(0, ukr[0], ukr[1], ukr[2], ukr[3]);
      f32x4 a = {0, 0, 0, 0};
      #pragma unroll
      for (int k = 0; k < 4; ++k) a = mfma(ukr[k], LD(u32x4, L_QV + v * 256 + 64 * k + 16 * g), a);
      const float sc = 0.08838834764831845f * 1.4426950408889634f;
      u32x4 gr = {pkbf(a[0] * sc, a[1] * sc), TL, pkbf(a[2] * sc, a[3] * sc), TH};
      if (r == 0) GSTC(WS_QA + ((b * 32 + h) * 128 + 32 * j + 4 * v + g) * 16, gr, ws);
    }
    STAMP(4);
    { u32x4 q[4];
      const u32 o = WS_QA + ((ba * 32 + 16 * hh + 2 * v) * 128 + lane) * 16;
      _Pragma("nounroll") for (int it = 0; it < SPIN_MAX; ++it) {
        POLL4(q[0], q[1], q[2], q[3], o, ws, 1024, 2048, 3072);
        if (__all(TAGOK(q[0]) & TAGOK(q[1]) & TAGOK(q[2]) & TAGOK(q[3]))) break;
      }
      #pragma unroll
      for (int k = 0; k < 4; ++k) { const u32 hd = 2 * v + (k >> 1), qd = 64 * (k & 1) + lane;
        LD(u32x2, L_Q + hd * 1024 + (((qd >> 1) ^ swz(hd)) << 4) + 8 * (qd & 1)) = u32x2{q[k][0], q[k][2]}; }
    }
    signal(2); STAMP(5); waitc(2, 8); waitc(3, 8);
    STAMP(6);
    if (v < 4) {
      f32x4 s0 = {0, 0, 0, 0}, s1 = {0, 0, 0, 0};
      const u32 fz = swz(r);
      #pragma unroll
      for (int t = 0; t < 16; ++t) { const u32 ch = ((4 * t + g) ^ fz) << 4;
        const u32x4 bq = LD(u32x4, L_Q + r * 1024 + ch);
        s0 = mfma(LD(u32x4, L_CKV + (32 * v + r) * 1024 + ch), bq, s0);
        s1 = mfma(LD(u32x4, L_CKV + (32 * v + 16 + r) * 1024 + ch), bq, s1); }
      float m = fmaxf(fmaxf(fmaxf(s0[0], s0[1]), fmaxf(s0[2], s0[3])), fmaxf(fmaxf(s1[0], s1[1]), fmaxf(s1[2], s1[3])));
      m = max32x(max16x(m));
      if (g == 0) LD(float, L_WM + 4 * (r * 4 + v)) = m;
      signal(4); waitc(4, 4);
      const f32x4 wm = LD(f32x4, L_WM + 16 * r);
      m = fmaxf(fmaxf(wm[0], wm[1]), fmaxf(wm[2], wm[3]));
      float l = 0;
      #pragma unroll
      for (int e = 0; e < 4; ++e) { s0[e] = __builtin_amdgcn_exp2f(s0[e] - m); s1[e] = __builtin_amdgcn_exp2f(s1[e] - m); l += s0[e] + s1[e]; }
      LD(u32x2, L_P + r * 256 + (((4 * v + (g >> 1)) ^ fz) << 4) + 8 * (g & 1)) = u32x2{pkbf(s0[0], s0[1]), pkbf(s0[2], s0[3])};
      LD(u32x2, L_P + r * 256 + (((4 * v + 2 + (g >> 1)) ^ fz) << 4) + 8 * (g & 1)) = u32x2{pkbf(s1[0], s1[1]), pkbf(s1[2], s1[3])};
      l = sum32x(sum16x(l));
      if (g == 0) LD(float, L_WL + 4 * (r * 4 + v)) = l;
      signal(5);
    }
    waitc(5, 4);
    STAMP(7);
    if (v == 7 && lane < 16) { const f32x4 wm = LD(f32x4, L_WM + 16 * lane), wl = LD(f32x4, L_WL + 16 * lane);
      u32x4 gr = {asu(fmaxf(fmaxf(wm[0], wm[1]), fmaxf(wm[2], wm[3]))), TL, asu(wl[0] + wl[1] + wl[2] + wl[3]), TH};
      GSTC(WS_ST + ((ba * 32 + 16 * hh + lane) * 64 + kt) * 16, gr, ws); }
    { f32x4 o[4] = {{0, 0, 0, 0}, {0, 0, 0, 0}, {0, 0, 0, 0}, {0, 0, 0, 0}};
      const u32 fz = swz(r), q4 = r >> 2, p4 = r & 3;
      #pragma unroll
      for (int t = 0; t < 4; ++t) {
        const u32x4 bp = LD(u32x4, L_P + r * 256 + (((4 * t + g) ^ fz) << 4));
        const u32 k0 = 32 * t + 8 * g + q4, k1 = k0 + 4;
        #pragma unroll
        for (int rb = 0; rb < 4; ++rb) { const u32 ch = 8 * v + 2 * rb + (p4 >> 1);
          const s16x4 a0 = __builtin_amdgcn_ds_read_tr16_b64_v4i16((LDSP(s16x4))(sm + L_CKV + k0 * 1024 + ((ch ^ swz(k0 & 15)) << 4) + 8 * (p4 & 1)));
          const s16x4 a1 = __builtin_amdgcn_ds_read_tr16_b64_v4i16((LDSP(s16x4))(sm + L_CKV + k1 * 1024 + ((ch ^ swz(k1 & 15)) << 4) + 8 * (p4 & 1)));
          const u32x2 b0 = __builtin_bit_cast(u32x2, a0), b1 = __builtin_bit_cast(u32x2, a1);
          o[rb] = mfma(u32x4{b0[0], b0[1], b1[0], b1[1]}, bp, o[rb]); }
      }
      #pragma unroll
      for (int rb = 0; rb < 4; ++rb) { u32x4 gr = {pkbf(o[rb][0], o[rb][1]), TL, pkbf(o[rb][2], o[rb][3]), TH};
        const u32 c0 = 64 * v + 16 * rb;
        GSTC(WS_PT + ((((ba * 32 + 16 * hh + r) * 4 + (c0 >> 7)) * 64 + kt) * 32 + ((c0 & 127) >> 2) + g) * 16, gr, ws); }
    }
    signal(7);
    STAMP(8);
    { u32x4 st, pt[4];
      const u32 qh = v & 1, sg = v >> 1;
      const u32 os = WS_ST + ((b * 32 + h) * 64 + lane) * 16;
      const u32 op = WS_PT + ((((b * 32 + h) * 4 + j) * 64 + 16 * sg + g) * 32 + 16 * qh + r) * 16;
      _Pragma("nounroll") for (int it = 0; it < SPIN_MAX; ++it) { POLL1(st, os, ws); if (__all(TAGOK(st))) break; }
      #pragma unroll
      for (int k = 0; k < 4; ++k) GLDC(pt[k], op + k * 2048, ws);
      const float M = wmax(asf(st[0]));
      const u32 ab = pkbf(__builtin_amdgcn_exp2f(asf(st[0]) - M), 0.f) & 0xFFFFu;
      const float Linv = 1.f / wsum(asf(ab << 16) * asf(st[2]));
      const u32 adup = ab | (ab << 16);
      u32 al[4];
      #pragma unroll
      for (int k = 0; k < 4; ++k) al[k] = __builtin_amdgcn_ds_bpermute(4 * (16 * sg + 4 * k + g), adup);
      WAIT4(0, pt[0], pt[1], pt[2], pt[3]);
      if (!__all(TAGOK(pt[0]) & TAGOK(pt[1]) & TAGOK(pt[2]) & TAGOK(pt[3]))) {
        _Pragma("nounroll") for (int it = 0; it < SPIN_MAX; ++it) {
          POLL4(pt[0], pt[1], pt[2], pt[3], op, ws, 2048, 4096, 6144);
          if (__all(TAGOK(pt[0]) & TAGOK(pt[1]) & TAGOK(pt[2]) & TAGOK(pt[3]))) break; } }
      STAMP(9);
      const u32 m0 = r == 0 ? 0xFFFFu : r == 1 ? 0xFFFF0000u : 0u, m2 = r == 2 ? 0xFFFFu : r == 3 ? 0xFFFF0000u : 0u;
      f32x4 a = {0, 0, 0, 0};
      #pragma unroll
      for (int k = 0; k < 4; ++k) a = mfma(pt[k], u32x4{al[k] & m0, 0u, al[k] & m2, 0u}, a);
      waitc(7, 8);
      if (r < 4) {
        #pragma unroll
        for (int e = 0; e < 4; ++e) LD(float, L_CPART + 16 * (4 * (16 * qh + 4 * g + e) + r) + 4 * sg) = a[e] * Linv; }
    }
    BAR();
    BAR();
    if (tid < 128) { const f32x4 p0 = LD(f32x4, L_VPART + 32 * tid), p1 = LD(f32x4, L_VPART + 32 * tid + 16);
      const float s = (p0[0] + p0[1]) + (p0[2] + p0[3]) + (p1[0] + p1[1]) + (p1[2] + p1[3]);
      LD(u32, L_CD + 4 * ((tid >> 6) * 64 + (tid & 3) * 16 + ((tid >> 2) & 15))) = pkbf(s, s); }
    BAR();
    BAR();
    if (tid < 256) { const f32x4 p = LD(f32x4, L_OPART + 16 * tid);
      u32x4 gr = {asu(p[0] + p[1]), TL, asu(p[2] + p[3]), TH};
      GSTC(WS_OP + (((b * 32 + (tid >> 3)) * 128 + prod) * 8 + (tid & 7)) * 16, gr, ws); }
    STAMP(10);
  } else {
    const u32 v = wid - 8;
    const u32 fb = wg >> 3, ks = wg & 7;
    const bool f1 = (wg < 176) && (v < 4);
    u32x4 f1r[4];
    if (f1) { const u32 col = min(64 * fb + 8 * (r & 7), 1376u - 8);
      const u32 o = (64 * ks + 16 * v + g) * 2752 + col * 2;
      const char* wsel = (r >> 3) ? w3 : w1;
      #pragma unroll
      for (int k = 0; k < 4; ++k) asm volatile("global_load_dwordx4 %0, %1, off" : "=v"(f1r[k]) : "v"(wsel + o + k * 4 * 2752)); }
    { const u32 o = (ba * 8192 + kt * 128 + 16 * v) * 1024;
      #pragma unroll
      for (int k = 0; k < 16; ++k) GLDS(o + k * 1024 + ((lane ^ swz(k)) << 4), ckv, smb + L_CKV + (16 * v + k) * 1024); }
    STAMP(1);
    GET_TAGS();
    if (f1) {
      waitc(0, 2);
      const u32x4 xd = LD(u32x4, L_XD + (r >> 3) * 2048 + 4 * (ks * 64 + g * 16 + 4 * v));
      WAIT4(16, f1r[0], f1r[1], f1r[2], f1r[3]);
      f32x4 a = {0, 0, 0, 0};
      #pragma unroll
      for (int k = 0; k < 4; ++k) a = mfma(f1r[k], DB(xd[k]), a);
      #pragma unroll
      for (int e = 0; e < 4; ++e) { const u32 row = 4 * g + e;
        LD(float, L_F1 + v * 1024 + 4 * ((((r >> 3) * 64 + (row & 7) * 8 + (r & 7)) << 1) + (row >> 3))) = a[e]; }
      signal(6);
      if (v < 2) { waitc(6, 4);
        f32x2 s = LD(f32x2, L_F1 + 8 * (v * 64 + lane));
        #pragma unroll
        for (int k = 1; k < 4; ++k) s += LD(f32x2, L_F1 + k * 1024 + 8 * (v * 64 + lane));
        u32x4 gr = {asu(s[0]), TL, asu(s[1]), TH};
        if (64 * fb + lane < 1376) GSTC(WS_FP + (((v * 22 + fb) * 8 + ks) * 64 + lane) * 16, gr, ws); }
    }
    WAITV(0);
    signal(3);
    STAMP(2);
    const bool f2 = prod < 86;
    if (v == 7) {
      float hv = 0.f;
      if (f2) { u32x4 q0, q1; const u32 f0 = 16 * prod;
        const u32 o = WS_FP + (((b * 22 + (f0 >> 6)) * 8 + g) * 64 + (f0 & 63) + r) * 16;
        _Pragma("nounroll") for (int it = 0; it < SPIN_MAX; ++it) { POLL2(q0, q1, o, o + 4096, ws);
          if (__all(TAGOK(q0) & TAGOK(q1))) break; }
        const float gg = sum32x(sum16x(asf(q0[0]) + asf(q1[0]))), uu = sum32x(sum16x(asf(q0[2]) + asf(q1[2])));
        hv = gg / (1.f + __expf(-gg)) * uu; }
      if (lane < 16) LD(u32, L_HD + 4 * lane) = pkbf(hv, hv);
    }
    u32x4 uvr[4], w2r[2], wor[16];
    const u32 ns = v & 3, kh = v >> 2;
    { const u32 o = (128 * j + 16 * v + g) * 8192 + h * 256 + r * 16;
      #pragma unroll
      for (int k = 0; k < 4; ++k) GLD(uvr[k], o + k * 32768, wuv); }
    { const u32 o = (min(16 * prod, 1376u - 16) + 8 * kh + g) * 1024 + ns * 256 + r * 16;
      GLD(w2r[0], o, w2); GLD(w2r[1], o + 4096, w2); }
    { const u32 o = (h * 128 + 64 * kh + g) * 1024 + ns * 256 + r * 16;
      #pragma unroll
      for (int k = 0; k < 16; ++k) GLD(wor[(k + 0) & 15], o + k * 4096, wo); }
    STAMP(3);
    BAR();
    STAMP(4);
    { WAIT4(18, uvr[0], uvr[1], uvr[2], uvr[3]);
      f32x4 a = {0, 0, 0, 0};
      #pragma unroll
      for (int k = 0; k < 4; ++k) { const f32x4 c = LD(f32x4, L_CPART + 16 * (16 * v + 4 * k + g)); const float s = (c[0] + c[1]) + (c[2] + c[3]);
        a = mfma(uvr[k], DB(pkbf(s, s)), a); }
      const u32 d0 = 32 * g + 16 * (r >> 3) + (r & 7);
      LD(float, L_VPART + 32 * d0 + 4 * v) = (r < 8) ? a[0] : a[2];
      LD(float, L_VPART + 32 * (d0 + 8) + 4 * v) = (r < 8) ? a[1] : a[3];
    }
    BAR();
    BAR();
    { u32x4 cd[4];
      #pragma unroll
      for (int k = 0; k < 4; ++k) cd[k] = LD(u32x4, L_CD + 4 * (kh * 64 + g * 16 + 4 * k));
      const u32 h0 = LD(u32, L_HD + 4 * (8 * kh + g)), h1 = LD(u32, L_HD + 4 * (8 * kh + 4 + g));
      WAIT2(0, w2r[0], w2r[1]);
      #pragma unroll
      for (int k = 0; k < 16; k += 4) WAIT4(0, wor[k], wor[k + 1], wor[k + 2], wor[k + 3]);
      f32x4 a0 = {0, 0, 0, 0}, a1 = {0, 0, 0, 0};
      #pragma unroll
      for (int k = 0; k < 16; k += 2) { a0 = mfma(wor[k], DB(cd[k >> 2][k & 3]), a0); a1 = mfma(wor[k + 1], DB(cd[k >> 2][(k & 3) + 1]), a1); }
      if (f2) { a0 = mfma(w2r[0], DB(h0), a0); a1 = mfma(w2r[1], DB(h1), a1); }
      a0 += a1;
      const u32 n0 = 128 * ns + 32 * g + 16 * (r >> 3) + (r & 7);
      LD(float, L_OPART + 8 * n0 + 4 * kh) = (r < 8) ? a0[0] : a0[2];
      LD(float, L_OPART + 8 * (n0 + 8) + 4 * kh) = (r < 8) ? a0[1] : a0[3];
    }
    BAR();
    STAMP(5);
  }
  if (j == 0) {
    u32x4 q, q2; const u32 o = WS_OP + (((b * 32 + h) * 128) * 8 + tid) * 16;
    u32 xv = 0; if (tid < 16) xv = *(const u16*)(x + b * 1024 + (16 * h + tid) * 2);
    const bool last = (wg == 0) && (wid < 2);
    _Pragma("nounroll") for (int it = 0; it < SPIN_MAX; ++it) { POLL1(q, o, ws); if (__all(TAGOK(q))) break; }
    float a = asf(q[0]), c = asf(q[2]);
    a += dppf<0x128>(a); c += dppf<0x128>(c);
    a = sum32x(sum16x(a)); c = sum32x(sum16x(c));
    if (lane < 8) LD(f32x2, L_FIN + wid * 64 + 8 * lane) = f32x2{a, c};
    if (last) { const u32 o2 = WS_OP + ((32 * 128 + wid * 64 + lane) * 8) * 16;
      _Pragma("nounroll") for (int it = 0; it < SPIN_MAX; ++it) { POLL1(q2, o2, ws); if (__all(TAGOK(q2))) break; } }
    BAR();
    if (tid < 16) { float s = asf(xv << 16);
      #pragma unroll
      for (int k = 0; k < 16; ++k) s += LD(float, L_FIN + k * 64 + 4 * tid);
      out[b * 512 + 16 * h + tid] = (u16)(pkbf(s, s) & 0xFFFFu); }
    if (wg == 0 && tid == 0) *(u64*)ws = E + 1;
  }
  STAMP(11);
}

extern "C" void kernel_launch(void* const* d_in, const int* in_sizes, int n_in, void* d_out, int out_size, void* d_ws, size_t ws_size, hipStream_t stream) {
  mla_ffn_fused<<<256, 1024, 0, stream>>>((const char*)d_in[0], (const char*)d_in[1], (const char*)d_in[2], (const char*)d_in[3], (const char*)d_in[4],
      (const char*)d_in[5], (const char*)d_in[7], (const char*)d_in[8], (const char*)d_in[9], (u16*)d_out, (char*)d_ws);
}
```

```cpp
#include <hip/hip_runtime.h>
typedef unsigned u32; typedef unsigned long long u64; typedef unsigned short u16;
typedef u32 u32x4 __attribute__((ext_vector_type(4)));
typedef u32 u32x2 __attribute__((ext_vector_type(2)));
typedef float f32x4 __attribute__((ext_vector_type(4)));
typedef float f32x2 __attribute__((ext_vector_type(2)));
typedef __bf16 bf16x8 __attribute__((ext_vector_type(8)));
typedef short s16x4 __attribute__((ext_vector_type(4)));
#define LDSP(T) __attribute__((address_space(3))) T*
constexpr u32 WS_QA = 4096;
constexpr u32 WS_FP = WS_QA + 131072;
constexpr u32 WS_ST = WS_FP + 360448;
constexpr u32 WS_OP = WS_ST + 65536;
constexpr u32 WS_PT = WS_OP + 1048576;
constexpr u32 L_CKV = 0, L_Q = 131072, L_P = 147456, L_XD = 151552, L_CNT = 155648, L_WM = 155776, L_WL = 156032,
              L_QV = 156288  , L_HD = 158336, L_F1 = 158464  , L_END = 162560;
constexpr u32 L_QPART = L_P;
constexpr u32 L_CPART = 0, L_VPART = 2048, L_CD = 6144, L_OPART = 8192, L_FIN = 16384;
constexpr int SPIN_MAX = 20000;

__device__ __forceinline__ f32x4 mfma(u32x4 a, u32x4 b, f32x4 c) {
  return __builtin_amdgcn_mfma_f32_16x16x32_bf16(__builtin_bit_cast(bf16x8, a), __builtin_bit_cast(bf16x8, b), c, 0, 0, 0); }
typedef __bf16 bf16x2 __attribute__((ext_vector_type(2)));
__device__ __forceinline__ u32 pkbf(float a, float b) { return __builtin_bit_cast(u32, __builtin_convertvector(f32x2{a, b}, bf16x2)); }
__device__ __forceinline__ float asf(u32 v) { return __builtin_bit_cast(float, v); }
__device__ __forceinline__ u32 asu(float v) { return __builtin_bit_cast(u32, v); }
#define GLD(dst, voff, sbase) asm volatile("global_load_dwordx4 %0, %1, %2" : "=v"(dst) : "v"(voff), "s"(sbase))
#define GLDC(dst, voff, sbase) asm volatile("global_load_dwordx4 %0, %1, %2 sc1" : "=v"(dst) : "v"(voff), "s"(sbase))
#define GSTC(voff, data, sbase) asm volatile("global_store_dwordx4 %0, %1, %2 sc1\n\ts_nop 2" :: "v"(voff), "v"(data), "s"(sbase) : "memory")
#define GLDS(voff, sbase, ldsaddr) asm volatile("s_mov_b32 m0, %2\n\ts_nop 0\n\tglobal_load_lds_dwordx4 %0, %1" :: "v"(voff), "s"(sbase), "s"(ldsaddr) : "memory")
#define WAITV(n) asm volatile("s_waitcnt vmcnt(%0)" :: "n"(n) : "memory")
#define TIE(r) asm volatile("" : "+v"(r))
#define SB() __builtin_amdgcn_sched_barrier(0)
#define WAIT4(n, a, b, c, d) do { asm volatile("s_waitcnt vmcnt(%4)" : "+v"(a), "+v"(b), "+v"(c), "+v"(d) : "n"(n) : "memory"); } while (0)
#define WAIT2(n, a, b) do { asm volatile("s_waitcnt vmcnt(%2)" : "+v"(a), "+v"(b) : "n"(n) : "memory"); } while (0)
#define WAIT1(n, a) do { asm volatile("s_waitcnt vmcnt(%1)" : "+v"(a) : "n"(n) : "memory"); } while (0)
#define POLL1(a, o0, sb) asm volatile("global_load_dwordx4 %0, %1, %2 sc1\n\ts_waitcnt vmcnt(0)" : "=&v"(a) : "v"(o0), "s"(sb) : "memory")
#define POLL2(a, b, o0, o1, sb) asm volatile("global_load_dwordx4 %0, %2, %4 sc1\n\tglobal_load_dwordx4 %1, %3, %4 sc1\n\ts_waitcnt vmcnt(0)" : "=&v"(a), "=&v"(b) : "v"(o0), "v"(o1), "s"(sb) : "memory")
#define POLL4(a, b, c, d, o0, sb, i1, i2, i3) asm volatile("global_load_dwordx4 %0, %4, %8 sc1\n\tglobal_load_dwordx4 %1, %5, %8 sc1\n\tglobal_load_dwordx4 %2, %6, %8 sc1\n\tglobal_load_dwordx4 %3, %7, %8 sc1\n\ts_waitcnt vmcnt(0)" \
   : "=&v"(a), "=&v"(b), "=&v"(c), "=&v"(d) : "v"(o0), "v"((o0) + (i1)), "v"((o0) + (i2)), "v"((o0) + (i3)), "s"(sb) : "memory")
#define TAGOK(q) (((q)[1] == TL) & ((q)[3] == TH))
#define BAR() asm volatile("s_waitcnt lgkmcnt(0)\n\ts_barrier" ::: "memory")
template <int CTRL> __device__ __forceinline__ float dppf(float v) { return asf(__builtin_amdgcn_update_dpp(0, asu(v), CTRL, 0xf, 0xf, true)); }
__device__ __forceinline__ float sum16x(float v) { auto r = __builtin_amdgcn_permlane16_swap(asu(v), asu(v), false, false); return asf(r[0]) + asf(r[1]); }
__device__ __forceinline__ float sum32x(float v) { auto r = __builtin_amdgcn_permlane32_swap(asu(v), asu(v), false, false); return asf(r[0]) + asf(r[1]); }
__device__ __forceinline__ float max16x(float v) { auto r = __builtin_amdgcn_permlane16_swap(asu(v), asu(v), false, false); return fmaxf(asf(r[0]), asf(r[1])); }
__device__ __forceinline__ float max32x(float v) { auto r = __builtin_amdgcn_permlane32_swap(asu(v), asu(v), false, false); return fmaxf(asf(r[0]), asf(r[1])); }
__device__ __forceinline__ float wsum(float v) { v += dppf<0xB1>(v); v += dppf<0x4E>(v); v += dppf<0x141>(v); v += dppf<0x140>(v); v = sum16x(v); return sum32x(v); }
__device__ __forceinline__ float wmax(float v) { v = fmaxf(v, dppf<0xB1>(v)); v = fmaxf(v, dppf<0x4E>(v)); v = fmaxf(v, dppf<0x141>(v)); v = fmaxf(v, dppf<0x140>(v)); v = max16x(v); return max32x(v); }
__device__ __forceinline__ u32 swz(u32 r) { return ((r & 3) << 2) | (((r >> 3) & 1) << 1); }


__global__ __launch_bounds__(1024) void mla_ffn_fused(const char* __restrict__ x, const char* __restrict__ ckv, const char* __restrict__ wq,
    const char* __restrict__ wuk, const char* __restrict__ wuv, const char* __restrict__ wo, const char* __restrict__ w1,
    const char* __restrict__ w3, const char* __restrict__ w2, u16* __restrict__ out, char* __restrict__ ws) {
  __shared__ __attribute__((aligned(1024))) char smem[L_END];
  LDSP(char) sm = (LDSP(char))smem;
#define LD(T, off) (*(LDSP(T))(sm + (off)))
#define LDV(T, off) (*(volatile LDSP(T))(sm + (off)))
  const u32 tid = threadIdx.x, lane = tid & 63;
  const u32 wid = __builtin_amdgcn_readfirstlane(tid >> 6);
  if (tid < 16) LD(u32, L_CNT + 4 * tid) = 0;
  BAR();
  const u32 smb = (u32)(size_t)sm;
  auto signal = [&](u32 k) { asm volatile("" ::: "memory"); if (lane == 0) asm volatile("ds_add_u32 %0, %1" :: "v"(smb + L_CNT + 4 * k), "v"(1u) : "memory"); asm volatile("" ::: "memory"); };
  auto waitc = [&](u32 k, u32 n) { _Pragma("nounroll") for (int it = 0; it < 4 * SPIN_MAX; ++it) { if (__builtin_amdgcn_readfirstlane(LDV(u32, L_CNT + 4 * k)) >= n) break; __builtin_amdgcn_s_sleep(1); } asm volatile("" ::: "memory"); };
  const u32 wg = blockIdx.x, xc = wg & 7, ii = wg >> 3;
  const u32 h = 4 * xc + (ii >> 3), b = ii & 1, j = (ii >> 1) & 3, prod = h * 4 + j;
  const u32 T = xc * 16 + (ii >> 1), ba = T >> 6, kt = T & 63, hh = ii & 1;
  const u32 r = lane & 15, g = lane >> 4;
  u64 E0, E; asm volatile("s_load_dwordx2 %0, %1, 0x0" : "=s"(E0) : "s"(ws));
  u32 TL, TH;
#define GET_TAGS() do { asm volatile("s_waitcnt lgkmcnt(0)\n\ts_mov_b64 %0, %1" : "=s"(E) : "s"(E0)); \
  u64 z = E + 0x9E3779B97F4A7C15ull; z = (z ^ (z >> 30)) * 0xBF58476D1CE4E5B9ull; z = (z ^ (z >> 27)) * 0x94D049BB133111EBull; z ^= z >> 31; \
  TL = (u32)z & 0xBFFFBFFFu; TH = (u32)(z >> 32) & 0xBFFFBFFFu; } while (0)
  u32x4 mk;
  { const u32 hm = (r & 1) ? 0xFFFF0000u : 0x0000FFFFu, t = (r & 7) >> 1; mk = u32x4{t == 0 ? hm : 0u, t == 1 ? hm : 0u, t == 2 ? hm : 0u, t == 3 ? hm : 0u}; }
#define DB(xd) (u32x4{(xd) & mk[0], (xd) & mk[1], (xd) & mk[2], (xd) & mk[3]})

  if (wid < 8) {
    const u32 v = wid;
    __builtin_amdgcn_s_setprio(3);
    u32x4 xr;
    if (v < 2) { GLD(xr, v * 1024 + lane * 16, x); }
    u32x4 wqr[16];
    const u32 wb = (v + ii) & 7;
    { const u32 o = (64 * wb + g) * 8192 + h * 256 + r * 16;
      #pragma unroll
      for (int k = 0; k < 16; ++k) GLD(wqr[k], o + k * 32768, wq); }
    u32x4 ukr[4];
    { const u32 o = (128 * j + 16 * v + r) * 8192 + h * 256 + g * 16;
      #pragma unroll
      for (int k = 0; k < 4; ++k) GLD(ukr[k], o + k * 64, wuk); }
    if (v < 2) {
      WAIT1(20, xr);
      float f[8];
      #pragma unroll
      for (int e = 0; e < 4; ++e) { f[2 * e] = asf(xr[e] << 16); f[2 * e + 1] = asf(xr[e] & 0xFFFF0000u); }
      float s = 0; for (int e = 0; e < 8; ++e) s += f[e];
      const float mean = wsum(s) * (1.f / 512);
      float q = 0; for (int e = 0; e < 8; ++e) { f[e] -= mean; q += f[e] * f[e]; }
      const float rstd = 1.f / sqrtf(wsum(q) * (1.f / 512) + 1e-5f);
      #pragma unroll
      for (int e = 0; e < 8; ++e) { const u32 d = pkbf(f[e] * rstd, f[e] * rstd);
        LD(u32, L_XD + v * 2048 + 4 * ((lane >> 3) * 64 + (e & 3) * 16 + 2 * (lane & 7) + (e >> 2))) = d; }
      signal(0);
    }
    waitc(0, 2);
    GET_TAGS();
    { u32x4 xd[4];
      #pragma unroll
      for (int k = 0; k < 4; ++k) xd[k] = LD(u32x4, L_XD + b * 2048 + 4 * (wb * 64 + g * 16 + 4 * k));
      f32x4 a0 = {0, 0, 0, 0}, a1 = {0, 0, 0, 0};
      #pragma unroll
      for (int k = 0; k < 4; ++k) {
        SB(); WAIT4(16 - 4 * k, wqr[4 * k], wqr[4 * k + 1], wqr[4 * k + 2], wqr[4 * k + 3]);
        a0 = mfma(wqr[4 * k], DB(xd[k][0]), a0); a1 = mfma(wqr[4 * k + 1], DB(xd[k][1]), a1);
        a0 = mfma(wqr[4 * k + 2], DB(xd[k][2]), a0); a1 = mfma(wqr[4 * k + 3], DB(xd[k][3]), a1);
      }
      a0 += a1;
      const u32 d0 = 32 * g + 16 * (r >> 3) + (r & 7);
      LD(float, L_QPART + v * 512 + 4 * d0) = (r < 8) ? a0[0] : a0[2];
      LD(float, L_QPART + v * 512 + 4 * d0 + 32) = (r < 8) ? a0[1] : a0[3];
    }
    signal(1); waitc(1, 8);
    { f32x2 s = LD(f32x2, L_QPART + 8 * lane);
      #pragma unroll
      for (int k = 1; k < 8; ++k) s += LD(f32x2, L_QPART + k * 512 + 8 * lane);
      LD(u32, L_QV + v * 256 + 4 * lane) = pkbf(s[0], s[1]);
      WAIT4(0, ukr[0], ukr[1], ukr[2], ukr[3]);
      f32x4 a = {0, 0, 0, 0};
      #pragma unroll
      for (int k = 0; k < 4; ++k) a = mfma(ukr[k], LD(u32x4, L_QV + v * 256 + 64 * k + 16 * g), a);
      const float sc = 0.08838834764831845f * 1.4426950408889634f;
      u32x4 gr = {pkbf(a[0] * sc, a[1] * sc), TL, pkbf(a[2] * sc, a[3] * sc), TH};
      if (r == 0) GSTC(WS_QA + ((b * 32 + h) * 128 + 32 * j + 4 * v + g) * 16, gr, ws);
    }
    { u32x4 q[4];
      const u32 o = WS_QA + ((ba * 32 + 16 * hh + 2 * v) * 128 + lane) * 16;
      _Pragma("nounroll") for (int it = 0; it < SPIN_MAX; ++it) {
        POLL4(q[0], q[1], q[2], q[3], o, ws, 1024, 2048, 3072);
        if (__all(TAGOK(q[0]) & TAGOK(q[1]) & TAGOK(q[2]) & TAGOK(q[3]))) break;
      }
      #pragma unroll
      for (int k = 0; k < 4; ++k) { const u32 hd = 2 * v + (k >> 1), qd = 64 * (k & 1) + lane;
        LD(u32x2, L_Q + hd * 1024 + (((qd >> 1) ^ swz(hd)) << 4) + 8 * (qd & 1)) = u32x2{q[k][0], q[k][2]}; }
    }
    signal(2); waitc(2, 8); waitc(3, 8);
    if (v < 4) {
      f32x4 s0 = {0, 0, 0, 0}, s1 = {0, 0, 0, 0};
      const u32 fz = swz(r);
      #pragma unroll
      for (int t = 0; t < 16; ++t) { const u32 ch = ((4 * t + g) ^ fz) << 4;
        const u32x4 bq = LD(u32x4, L_Q + r * 1024 + ch);
        s0 = mfma(LD(u32x4, L_CKV + (32 * v + r) * 1024 + ch), bq, s0);
        s1 = mfma(LD(u32x4, L_CKV + (32 * v + 16 + r) * 1024 + ch), bq, s1); }
      float m = fmaxf(fmaxf(fmaxf(s0[0], s0[1]), fmaxf(s0[2], s0[3])), fmaxf(fmaxf(s1[0], s1[1]), fmaxf(s1[2], s1[3])));
      m = max32x(max16x(m));
      if (g == 0) LD(float, L_WM + 4 * (r * 4 + v)) = m;
      signal(4); waitc(4, 4);
      const f32x4 wm = LD(f32x4, L_WM + 16 * r);
      m = fmaxf(fmaxf(wm[0], wm[1]), fmaxf(wm[2], wm[3]));
      float l = 0;
      #pragma unroll
      for (int e = 0; e < 4; ++e) { s0[e] = __builtin_amdgcn_exp2f(s0[e] - m); s1[e] = __builtin_amdgcn_exp2f(s1[e] - m); l += s0[e] + s1[e]; }
      LD(u32x2, L_P + r * 256 + (((4 * v + (g >> 1)) ^ fz) << 4) + 8 * (g & 1)) = u32x2{pkbf(s0[0], s0[1]), pkbf(s0[2], s0[3])};
      LD(u32x2, L_P + r * 256 + (((4 * v + 2 + (g >> 1)) ^ fz) << 4) + 8 * (g & 1)) = u32x2{pkbf(s1[0], s1[1]), pkbf(s1[2], s1[3])};
      l = sum32x(sum16x(l));
      if (g == 0) LD(float, L_WL + 4 * (r * 4 + v)) = l;
      signal(5);
    }
    waitc(5, 4);
    if (v == 7 && lane < 16) { const f32x4 wm = LD(f32x4, L_WM + 16 * lane), wl = LD(f32x4, L_WL + 16 * lane);
      u32x4 gr = {asu(fmaxf(fmaxf(wm[0], wm[1]), fmaxf(wm[2], wm[3]))), TL, asu(wl[0] + wl[1] + wl[2] + wl[3]), TH};
      GSTC(WS_ST + ((ba * 32 + 16 * hh + lane) * 64 + kt) * 16, gr, ws); }
    { f32x4 o[4] = {{0, 0, 0, 0}, {0, 0, 0, 0}, {0, 0, 0, 0}, {0, 0, 0, 0}};
      const u32 fz = swz(r), q4 = r >> 2, p4 = r & 3;
      #pragma unroll
      for (int t = 0; t < 4; ++t) {
        const u32x4 bp = LD(u32x4, L_P + r * 256 + (((4 * t + g) ^ fz) << 4));
        const u32 k0 = 32 * t + 8 * g + q4, k1 = k0 + 4;
        #pragma unroll
        for (int rb = 0; rb < 4; ++rb) { const u32 ch = 8 * v + 2 * rb + (p4 >> 1);
          const s16x4 a0 = __builtin_amdgcn_ds_read_tr16_b64_v4i16((LDSP(s16x4))(sm + L_CKV + k0 * 1024 + ((ch ^ swz(k0 & 15)) << 4) + 8 * (p4 & 1)));
          const s16x4 a1 = __builtin_amdgcn_ds_read_tr16_b64_v4i16((LDSP(s16x4))(sm + L_CKV + k1 * 1024 + ((ch ^ swz(k1 & 15)) << 4) + 8 * (p4 & 1)));
          const u32x2 b0 = __builtin_bit_cast(u32x2, a0), b1 = __builtin_bit_cast(u32x2, a1);
          o[rb] = mfma(u32x4{b0[0], b0[1], b1[0], b1[1]}, bp, o[rb]); }
      }
      #pragma unroll
      for (int rb = 0; rb < 4; ++rb) { u32x4 gr = {pkbf(o[rb][0], o[rb][1]), TL, pkbf(o[rb][2], o[rb][3]), TH};
        const u32 c0 = 64 * v + 16 * rb;
        GSTC(WS_PT + ((((ba * 32 + 16 * hh + r) * 4 + (c0 >> 7)) * 64 + kt) * 32 + ((c0 & 127) >> 2) + g) * 16, gr, ws); }
    }
    signal(7);
    { u32x4 st, pt[4];
      const u32 qh = v & 1, sg = v >> 1;
      const u32 os = WS_ST + ((b * 32 + h) * 64 + lane) * 16;
      const u32 op = WS_PT + ((((b * 32 + h) * 4 + j) * 64 + 16 * sg + g) * 32 + 16 * qh + r) * 16;
      _Pragma("nounroll") for (int it = 0; it < SPIN_MAX; ++it) { POLL1(st, os, ws); if (__all(TAGOK(st))) break; }
      #pragma unroll
      for (int k = 0; k < 4; ++k) GLDC(pt[k], op + k * 2048, ws);
      const float M = wmax(asf(st[0]));
      const u32 ab = pkbf(__builtin_amdgcn_exp2f(asf(st[0]) - M), 0.f) & 0xFFFFu;
      const float Linv = 1.f / wsum(asf(ab << 16) * asf(st[2]));
      const u32 adup = ab | (ab << 16);
      u32 al[4];
      #pragma unroll
      for (int k = 0; k < 4; ++k) al[k] = __builtin_amdgcn_ds_bpermute(4 * (16 * sg + 4 * k + g), adup);
      WAIT4(0, pt[0], pt[1], pt[2], pt[3]);
      if (!__all(TAGOK(pt[0]) & TAGOK(pt[1]) & TAGOK(pt[2]) & TAGOK(pt[3]))) {
        _Pragma("nounroll") for (int it = 0; it < SPIN_MAX; ++it) {
          POLL4(pt[0], pt[1], pt[2], pt[3], op, ws, 2048, 4096, 6144);
          if (__all(TAGOK(pt[0]) & TAGOK(pt[1]) & TAGOK(pt[2]) & TAGOK(pt[3]))) break; } }
      const u32 m0 = r == 0 ? 0xFFFFu : r == 1 ? 0xFFFF0000u : 0u, m2 = r == 2 ? 0xFFFFu : r == 3 ? 0xFFFF0000u : 0u;
      f32x4 a = {0, 0, 0, 0};
      #pragma unroll
      for (int k = 0; k < 4; ++k) a = mfma(pt[k], u32x4{al[k] & m0, 0u, al[k] & m2, 0u}, a);
      waitc(7, 8);
      if (r < 4) {
        #pragma unroll
        for (int e = 0; e < 4; ++e) LD(float, L_CPART + 16 * (4 * (16 * qh + 4 * g + e) + r) + 4 * sg) = a[e] * Linv; }
    }
    BAR();
    BAR();
    if (tid < 128) { const f32x4 p0 = LD(f32x4, L_VPART + 32 * tid), p1 = LD(f32x4, L_VPART + 32 * tid + 16);
      const float s = (p0[0] + p0[1]) + (p0[2] + p0[3]) + (p1[0] + p1[1]) + (p1[2] + p1[3]);
      LD(u32, L_CD + 4 * ((tid >> 6) * 64 + (tid & 3) * 16 + ((tid >> 2) & 15))) = pkbf(s, s); }
    BAR();
    BAR();
    if (tid < 256) { const f32x4 p = LD(f32x4, L_OPART + 16 * tid);
      u32x4 gr = {asu(p[0] + p[1]), TL, asu(p[2] + p[3]), TH};
      GSTC(WS_OP + (((b * 32 + (tid >> 3)) * 128 + prod) * 8 + (tid & 7)) * 16, gr, ws); }
  } else {
    const u32 v = wid - 8;
    const u32 fb = wg >> 3, ks = wg & 7;
    const bool f1 = (wg < 176) && (v < 4);
    u32x4 f1r[4];
    if (f1) { const u32 col = min(64 * fb + 8 * (r & 7), 1376u - 8);
      const u32 o = (64 * ks + 16 * v + g) * 2752 + col * 2;
      const char* wsel = (r >> 3) ? w3 : w1;
      #pragma unroll
      for (int k = 0; k < 4; ++k) asm volatile("global_load_dwordx4 %0, %1, off" : "=v"(f1r[k]) : "v"(wsel + o + k * 4 * 2752)); }
    { const u32 o = (ba * 8192 + kt * 128 + 16 * v) * 1024;
      #pragma unroll
      for (int k = 0; k < 16; ++k) GLDS(o + k * 1024 + ((lane ^ swz(k)) << 4), ckv, smb + L_CKV + (16 * v + k) * 1024); }
    GET_TAGS();
    if (f1) {
      waitc(0, 2);
      const u32x4 xd = LD(u32x4, L_XD + (r >> 3) * 2048 + 4 * (ks * 64 + g * 16 + 4 * v));
      WAIT4(16, f1r[0], f1r[1], f1r[2], f1r[3]);
      f32x4 a = {0, 0, 0, 0};
      #pragma unroll
      for (int k = 0; k < 4; ++k) a = mfma(f1r[k], DB(xd[k]), a);
      #pragma unroll
      for (int e = 0; e < 4; ++e) { const u32 row = 4 * g + e;
        LD(float, L_F1 + v * 1024 + 4 * ((((r >> 3) * 64 + (row & 7) * 8 + (r & 7)) << 1) + (row >> 3))) = a[e]; }
      signal(6);
      if (v < 2) { waitc(6, 4);
        f32x2 s = LD(f32x2, L_F1 + 8 * (v * 64 + lane));
        #pragma unroll
        for (int k = 1; k < 4; ++k) s += LD(f32x2, L_F1 + k * 1024 + 8 * (v * 64 + lane));
        u32x4 gr = {asu(s[0]), TL, asu(s[1]), TH};
        if (64 * fb + lane < 1376) GSTC(WS_FP + (((v * 22 + fb) * 8 + ks) * 64 + lane) * 16, gr, ws); }
    }
    WAITV(0);
    signal(3);
    const bool f2 = prod < 86;
    if (v == 7) {
      float hv = 0.f;
      if (f2) { u32x4 q0, q1; const u32 f0 = 16 * prod;
        const u32 o = WS_FP + (((b * 22 + (f0 >> 6)) * 8 + g) * 64 + (f0 & 63) + r) * 16;
        _Pragma("nounroll") for (int it = 0; it < SPIN_MAX; ++it) { POLL2(q0, q1, o, o + 4096, ws);
          if (__all(TAGOK(q0) & TAGOK(q1))) break; }
        const float gg = sum32x(sum16x(asf(q0[0]) + asf(q1[0]))), uu = sum32x(sum16x(asf(q0[2]) + asf(q1[2])));
        hv = gg / (1.f + __expf(-gg)) * uu; }
      if (lane < 16) LD(u32, L_HD + 4 * lane) = pkbf(hv, hv);
    }
    u32x4 uvr[4], w2r[2], wor[16];
    const u32 ns = v & 3, kh = v >> 2;
    { const u32 o = (128 * j + 16 * v + g) * 8192 + h * 256 + r * 16;
      #pragma unroll
      for (int k = 0; k < 4; ++k) GLD(uvr[k], o + k * 32768, wuv); }
    { const u32 o = (min(16 * prod, 1376u - 16) + 8 * kh + g) * 1024 + ns * 256 + r * 16;
      GLD(w2r[0], o, w2); GLD(w2r[1], o + 4096, w2); }
    { const u32 o = (h * 128 + 64 * kh + g) * 1024 + ns * 256 + r * 16;
      #pragma unroll
      for (int k = 0; k < 16; ++k) GLD(wor[(k + 0) & 15], o + k * 4096, wo); }
    BAR();
    { WAIT4(18, uvr[0], uvr[1], uvr[2], uvr[3]);
      f32x4 a = {0, 0, 0, 0};
      #pragma unroll
      for (int k = 0; k < 4; ++k) { const f32x4 c = LD(f32x4, L_CPART + 16 * (16 * v + 4 * k + g)); const float s = (c[0] + c[1]) + (c[2] + c[3]);
        a = mfma(uvr[k], DB(pkbf(s, s)), a); }
      const u32 d0 = 32 * g + 16 * (r >> 3) + (r & 7);
      LD(float, L_VPART + 32 * d0 + 4 * v) = (r < 8) ? a[0] : a[2];
      LD(float, L_VPART + 32 * (d0 + 8) + 4 * v) = (r < 8) ? a[1] : a[3];
    }
    BAR();
    BAR();
    { u32x4 cd[4];
      #pragma unroll
      for (int k = 0; k < 4; ++k) cd[k] = LD(u32x4, L_CD + 4 * (kh * 64 + g * 16 + 4 * k));
      const u32 h0 = LD(u32, L_HD + 4 * (8 * kh + g)), h1 = LD(u32, L_HD + 4 * (8 * kh + 4 + g));
      WAIT2(0, w2r[0], w2r[1]);
      #pragma unroll
      for (int k = 0; k < 16; k += 4) WAIT4(0, wor[k], wor[k + 1], wor[k + 2], wor[k + 3]);
      f32x4 a0 = {0, 0, 0, 0}, a1 = {0, 0, 0, 0};
      #pragma unroll
      for (int k = 0; k < 16; k += 2) { a0 = mfma(wor[k], DB(cd[k >> 2][k & 3]), a0); a1 = mfma(wor[k + 1], DB(cd[k >> 2][(k & 3) + 1]), a1); }
      if (f2) { a0 = mfma(w2r[0], DB(h0), a0); a1 = mfma(w2r[1], DB(h1), a1); }
      a0 += a1;
      const u32 n0 = 128 * ns + 32 * g + 16 * (r >> 3) + (r & 7);
      LD(float, L_OPART + 8 * n0 + 4 * kh) = (r < 8) ? a0[0] : a0[2];
      LD(float, L_OPART + 8 * (n0 + 8) + 4 * kh) = (r < 8) ? a0[1] : a0[3];
    }
    BAR();
  }
  if (j == 0) {
    u32x4 q, q2; const u32 o = WS_OP + (((b * 32 + h) * 128) * 8 + tid) * 16;
    u32 xv = 0; if (tid < 16) xv = *(const u16*)(x + b * 1024 + (16 * h + tid) * 2);
    const bool last = (wg == 0) && (wid < 2);
    _Pragma("nounroll") for (int it = 0; it < SPIN_MAX; ++it) { POLL1(q, o, ws); if (__all(TAGOK(q))) break; }
    float a = asf(q[0]), c = asf(q[2]);
    a += dppf<0x128>(a); c += dppf<0x128>(c);
    a = sum32x(sum16x(a)); c = sum32x(sum16x(c));
    if (lane < 8) LD(f32x2, L_FIN + wid * 64 + 8 * lane) = f32x2{a, c};
    if (last) { const u32 o2 = WS_OP + ((32 * 128 + wid * 64 + lane) * 8) * 16;
      _Pragma("nounroll") for (int it = 0; it < SPIN_MAX; ++it) { POLL1(q2, o2, ws); if (__all(TAGOK(q2))) break; } }
    BAR();
    if (tid < 16) { float s = asf(xv << 16);
      #pragma unroll
      for (int k = 0; k < 16; ++k) s += LD(float, L_FIN + k * 64 + 4 * tid);
      out[b * 512 + 16 * h + tid] = (u16)(pkbf(s, s) & 0xFFFFu); }
    if (wg == 0 && tid == 0) *(u64*)ws = E + 1;
  }
}

extern "C" void kernel_launch(void* const* d_in, const int* in_sizes, int n_in, void* d_out, int out_size, void* d_ws, size_t ws_size, hipStream_t stream) {
  mla_ffn_fused<<<256, 1024, 0, stream>>>((const char*)d_in[0], (const char*)d_in[1], (const char*)d_in[2], (const char*)d_in[3], (const char*)d_in[4],
      (const char*)d_in[5], (const char*)d_in[7], (const char*)d_in[8], (const char*)d_in[9], (u16*)d_out, (char*)d_ws);
}
```

```cpp
#include <hip/hip_runtime.h>
typedef unsigned u32; typedef unsigned long long u64; typedef unsigned short u16;
typedef u32 u32x4 __attribute__((ext_vector_type(4)));
typedef u32 u32x2 __attribute__((ext_vector_type(2)));
typedef float f32x4 __attribute__((ext_vector_type(4)));
typedef float f32x2 __attribute__((ext_vector_type(2)));
typedef __bf16 bf16x8 __attribute__((ext_vector_type(8)));
typedef short s16x4 __attribute__((ext_vector_type(4)));
#define LDSP(T) __attribute__((address_space(3))) T*
#define NSTAMP 0
constexpr u32 WS_QA = 4096;
constexpr u32 WS_FP = WS_QA + 131072;
constexpr u32 WS_ST = WS_FP + 360448;
constexpr u32 WS_OP = WS_ST + 65536;
constexpr u32 WS_PT = WS_OP + 1048576;
constexpr u32 L_CKV = 0, L_Q = 131072, L_P = 147456, L_XD = 151552, L_CNT = 155648, L_WM = 155776, L_WL = 156032,
              L_QV = 156288  , L_HD = 158336, L_F1 = 158464  , L_END = 162560;
constexpr u32 L_QPART = L_P;
constexpr u32 L_CPART = 0, L_VPART = 2048, L_CD = 6144, L_OPART = 8192, L_FIN = 16384;
constexpr int SPIN_MAX = 20000;

__device__ __forceinline__ f32x4 mfma(u32x4 a, u32x4 b, f32x4 c) {
  return __builtin_amdgcn_mfma_f32_16x16x32_bf16(__builtin_bit_cast(bf16x8, a), __builtin_bit_cast(bf16x8, b), c, 0, 0, 0); }
typedef __bf16 bf16x2 __attribute__((ext_vector_type(2)));
__device__ __forceinline__ u32 pkbf(float a, float b) { return __builtin_bit_cast(u32, __builtin_convertvector(f32x2{a, b}, bf16x2)); }
__device__ __forceinline__ float asf(u32 v) { return __builtin_bit_cast(float, v); }
__device__ __forceinline__ u32 asu(float v) { return __builtin_bit_cast(u32, v); }
#define GLD(dst, voff, sbase) asm volatile("global_load_dwordx4 %0, %1, %2" : "=v"(dst) : "v"(voff), "s"(sbase))
#define GLDC(dst, voff, sbase) asm volatile("global_load_dwordx4 %0, %1, %2 sc1" : "=v"(dst) : "v"(voff), "s"(sbase))
#define GSTC(voff, data, sbase) asm volatile("global_store_dwordx4 %0, %1, %2 sc1\n\ts_nop 2" :: "v"(voff), "v"(data), "s"(sbase) : "memory")
#define GLDS(voff, sbase, ldsaddr) asm volatile("s_mov_b32 m0, %2\n\ts_nop 0\n\tglobal_load_lds_dwordx4 %0, %1" :: "v"(voff), "s"(sbase), "s"(ldsaddr) : "memory")
#define WAITV(n) asm volatile("s_waitcnt vmcnt(%0)" :: "n"(n) : "memory")
#define TIE(r) asm volatile("" : "+v"(r))
#define SB() __builtin_amdgcn_sched_barrier(0)
#define WAIT4(n, a, b, c, d) do { asm volatile("s_waitcnt vmcnt(%4)" : "+v"(a), "+v"(b), "+v"(c), "+v"(d) : "n"(n) : "memory"); } while (0)
#define WAIT2(n, a, b) do { asm volatile("s_waitcnt vmcnt(%2)" : "+v"(a), "+v"(b) : "n"(n) : "memory"); } while (0)
#define WAIT1(n, a) do { asm volatile("s_waitcnt vmcnt(%1)" : "+v"(a) : "n"(n) : "memory"); } while (0)
#define POLL1(a, o0, sb) asm volatile("global_load_dwordx4 %0, %1, %2 sc1\n\ts_waitcnt vmcnt(0)" : "=&v"(a) : "v"(o0), "s"(sb) : "memory")
#define POLL2(a, b, o0, o1, sb) asm volatile("global_load_dwordx4 %0, %2, %4 sc1\n\tglobal_load_dwordx4 %1, %3, %4 sc1\n\ts_waitcnt vmcnt(0)" : "=&v"(a), "=&v"(b) : "v"(o0), "v"(o1), "s"(sb) : "memory")
#define POLL4(a, b, c, d, o0, sb, i1, i2, i3) asm volatile("global_load_dwordx4 %0, %4, %8 sc1\n\tglobal_load_dwordx4 %1, %5, %8 sc1\n\tglobal_load_dwordx4 %2, %6, %8 sc1\n\tglobal_load_dwordx4 %3, %7, %8 sc1\n\ts_waitcnt vmcnt(0)" \
   : "=&v"(a), "=&v"(b), "=&v"(c), "=&v"(d) : "v"(o0), "v"((o0) + (i1)), "v"((o0) + (i2)), "v"((o0) + (i3)), "s"(sb) : "memory")
#define TAGOK(q) (((q)[1] == TL) & ((q)[3] == TH))
#define BAR() asm volatile("s_waitcnt lgkmcnt(0)\n\ts_barrier" ::: "memory")
template <int CTRL> __device__ __forceinline__ float dppf(float v) { return asf(__builtin_amdgcn_update_dpp(0, asu(v), CTRL, 0xf, 0xf, true)); }
__device__ __forceinline__ float sum16x(float v) { auto r = __builtin_amdgcn_permlane16_swap(asu(v), asu(v), false, false); return asf(r[0]) + asf(r[1]); }
__device__ __forceinline__ float sum32x(float v) { auto r = __builtin_amdgcn_permlane32_swap(asu(v), asu(v), false, false); return asf(r[0]) + asf(r[1]); }
__device__ __forceinline__ float max16x(float v) { auto r = __builtin_amdgcn_permlane16_swap(asu(v), asu(v), false, false); return fmaxf(asf(r[0]), asf(r[1])); }
__device__ __forceinline__ float max32x(float v) { auto r = __builtin_amdgcn_permlane32_swap(asu(v), asu(v), false, false); return fmaxf(asf(r[0]), asf(r[1])); }
__device__ __forceinline__ float wsum(float v) { v += dppf<0xB1>(v); v += dppf<0x4E>(v); v += dppf<0x141>(v); v += dppf<0x140>(v); v = sum16x(v); return sum32x(v); }
__device__ __forceinline__ float wmax(float v) { v = fmaxf(v, dppf<0xB1>(v)); v = fmaxf(v, dppf<0x4E>(v)); v = fmaxf(v, dppf<0x141>(v)); v = fmaxf(v, dppf<0x140>(v)); v = max16x(v); return max32x(v); }
__device__ __forceinline__ u32 swz(u32 r) { return ((r & 3) << 2) | (((r >> 3) & 1) << 1); }

#define STAMP(k) do {} while (0)

__global__ __launch_bounds__(1024) void mla_ffn_fused(const char* __restrict__ x, const char* __restrict__ ckv, const char* __restrict__ wq,
    const char* __restrict__ wuk, const char* __restrict__ wuv, const char* __restrict__ wo, const char* __restrict__ w1,
    const char* __restrict__ w3, const char* __restrict__ w2, u16* __restrict__ out, char* __restrict__ ws) {
  __shared__ __attribute__((aligned(1024))) char smem[L_END];
  LDSP(char) sm = (LDSP(char))smem;
#define LD(T, off) (*(LDSP(T))(sm + (off)))
#define LDV(T, off) (*(volatile LDSP(T))(sm + (off)))
  const u32 tid = threadIdx.x, lane = tid & 63;
  const u32 wid = __builtin_amdgcn_readfirstlane(tid >> 6);
  const u64 tstart = __builtin_amdgcn_s_memrealtime();
  STAMP(0);
  if (tid < 16) LD(u32, L_CNT + 4 * tid) = 0;
  BAR();
  const u32 smb = (u32)(size_t)sm;
  auto signal = [&](u32 k) { asm volatile("" ::: "memory"); if (lane == 0) asm volatile("ds_add_u32 %0, %1" :: "v"(smb + L_CNT + 4 * k), "v"(1u) : "memory"); asm volatile("" ::: "memory"); };
  auto waitc = [&](u32 k, u32 n) { _Pragma("nounroll") for (int it = 0; it < 4 * SPIN_MAX; ++it) { if (__builtin_amdgcn_readfirstlane(LDV(u32, L_CNT + 4 * k)) >= n) break; __builtin_amdgcn_s_sleep(1); } asm volatile("" ::: "memory"); };
  const u32 wg = blockIdx.x, xc = wg & 7, ii = wg >> 3;
  const u32 h = 4 * xc + (ii >> 3), b = ii & 1, j = (ii >> 1) & 3, prod = h * 4 + j;
  const u32 T = xc * 16 + (ii >> 1), ba = T >> 6, kt = T & 63, hh = ii & 1;
  const u32 r = lane & 15, g = lane >> 4;
  u64 E0, E; asm volatile("s_load_dwordx2 %0, %1, 0x0" : "=s"(E0) : "s"(ws));
  u32 TL, TH;
#define GET_TAGS() do { asm volatile("s_waitcnt lgkmcnt(0)\n\ts_mov_b64 %0, %1" : "=s"(E) : "s"(E0)); \
  u64 z = E + 0x9E3779B97F4A7C15ull; z = (z ^ (z >> 30)) * 0xBF58476D1CE4E5B9ull; z = (z ^ (z >> 27)) * 0x94D049BB133111EBull; z ^= z >> 31; \
  TL = (u32)z & 0xBFFFBFFFu; TH = (u32)(z >> 32) & 0xBFFFBFFFu; } while (0)
  u32x4 mk;
  { const u32 hm = (r & 1) ? 0xFFFF0000u : 0x0000FFFFu, t = (r & 7) >> 1; mk = u32x4{t == 0 ? hm : 0u, t == 1 ? hm : 0u, t == 2 ? hm : 0u, t == 3 ? hm : 0u}; }
#define DB(xd) (u32x4{(xd) & mk[0], (xd) & mk[1], (xd) & mk[2], (xd) & mk[3]})

  if (wid < 8) {
    const u32 v = wid;
    __builtin_amdgcn_s_setprio(3);
    u32x4 xr;
    if (v < 2) { GLD(xr, v * 1024 + lane * 16, x); }
    u32x4 wqr[16];
    const u32 wb = (v + ii) & 7;
    { const u32 o = (64 * wb + g) * 8192 + h * 256 + r * 16;
      #pragma unroll
      for (int k = 0; k < 16; ++k) GLD(wqr[k], o + k * 32768, wq); }
    u32x4 ukr[4];
    { const u32 o = (128 * j + 16 * v + r) * 8192 + h * 256 + g * 16;
      #pragma unroll
      for (int k = 0; k < 4; ++k) GLD(ukr[k], o + k * 64, wuk); }
    STAMP(1);
    if (v < 2) {
      WAIT1(20, xr);
      float f[8];
      #pragma unroll
      for (int e = 0; e < 4; ++e) { f[2 * e] = asf(xr[e] << 16); f[2 * e + 1] = asf(xr[e] & 0xFFFF0000u); }
      float s = 0; for (int e = 0; e < 8; ++e) s += f[e];
      const float mean = wsum(s) * (1.f / 512);
      float q = 0; for (int e = 0; e < 8; ++e) { f[e] -= mean; q += f[e] * f[e]; }
      const float rstd = 1.f / sqrtf(wsum(q) * (1.f / 512) + 1e-5f);
      #pragma unroll
      for (int e = 0; e < 8; ++e) { const u32 d = pkbf(f[e] * rstd, f[e] * rstd);
        LD(u32, L_XD + v * 2048 + 4 * ((lane >> 3) * 64 + (e & 3) * 16 + 2 * (lane & 7) + (e >> 2))) = d; }
      signal(0);
    }
    waitc(0, 2);
    GET_TAGS();
    STAMP(2);
    { u32x4 xd[4];
      #pragma unroll
      for (int k = 0; k < 4; ++k) xd[k] = LD(u32x4, L_XD + b * 2048 + 4 * (wb * 64 + g * 16 + 4 * k));
      f32x4 a0 = {0, 0, 0, 0}, a1 = {0, 0, 0, 0};
      #pragma unroll
      for (int k = 0; k < 4; ++k) {
        SB(); WAIT4(16 - 4 * k, wqr[4 * k], wqr[4 * k + 1], wqr[4 * k + 2], wqr[4 * k + 3]);
        a0 = mfma(wqr[4 * k], DB(xd[k][0]), a0); a1 = mfma(wqr[4 * k + 1], DB(xd[k][1]), a1);
        a0 = mfma(wqr[4 * k + 2], DB(xd[k][2]), a0); a1 = mfma(wqr[4 * k + 3], DB(xd[k][3]), a1);
      }
      a0 += a1;
      const u32 d0 = 32 * g + 16 * (r >> 3) + (r & 7);
      LD(float, L_QPART + v * 512 + 4 * d0) = (r < 8) ? a0[0] : a0[2];
      LD(float, L_QPART + v * 512 + 4 * d0 + 32) = (r < 8) ? a0[1] : a0[3];
    }
    signal(1); waitc(1, 8);
    STAMP(3);
    { f32x2 s = LD(f32x2, L_QPART + 8 * lane);
      #pragma unroll
      for (int k = 1; k < 8; ++k) s += LD(f32x2, L_QPART + k * 512 + 8 * lane);
      LD(u32, L_QV + v * 256 + 4 * lane) = pkbf(s[0], s[1]);
      WAIT4(0, ukr[0], ukr[1], ukr[2], ukr[3]);
      f32x4 a = {0, 0, 0, 0};
      #pragma unroll
      for (int k = 0; k < 4; ++k) a = mfma(ukr[k], LD(u32x4, L_QV + v * 256 + 64 * k + 16 * g), a);
      const float sc = 0.08838834764831845f * 1.4426950408889634f;
      u32x4 gr = {pkbf(a[0] * sc, a[1] * sc), TL, pkbf(a[2] * sc, a[3] * sc), TH};
      if (r == 0) GSTC(WS_QA + ((b * 32 + h) * 128 + 32 * j + 4 * v + g) * 16, gr, ws);
    }
    STAMP(4);
    { u32x4 q[4];
      const u32 o = WS_QA + ((ba * 32 + 16 * hh + 2 * v) * 128 + lane) * 16;
      _Pragma("nounroll") for (int it = 0; it < SPIN_MAX; ++it) {
        POLL4(q[0], q[1], q[2], q[3], o, ws, 1024, 2048, 3072);
        if (__all(TAGOK(q[0]) & TAGOK(q[1]) & TAGOK(q[2]) & TAGOK(q[3]))) break;
      }
      #pragma unroll
      for (int k = 0; k < 4; ++k) { const u32 hd = 2 * v + (k >> 1), qd = 64 * (k & 1) + lane;
        LD(u32x2, L_Q + hd * 1024 + (((qd >> 1) ^ swz(hd)) << 4) + 8 * (qd & 1)) = u32x2{q[k][0], q[k][2]}; }
    }
    signal(2); STAMP(5); waitc(2, 8); waitc(3, 8);
    STAMP(6);
    if (v < 4) {
      f32x4 s0 = {0, 0, 0, 0}, s1 = {0, 0, 0, 0};
      const u32 fz = swz(r);
      #pragma unroll
      for (int t = 0; t < 16; ++t) { const u32 ch = ((4 * t + g) ^ fz) << 4;
        const u32x4 bq = LD(u32x4, L_Q + r * 1024 + ch);
        s0 = mfma(LD(u32x4, L_CKV + (32 * v + r) * 1024 + ch), bq, s0);
        s1 = mfma(LD(u32x4, L_CKV + (32 * v + 16 + r) * 1024 + ch), bq, s1); }
      float m = fmaxf(fmaxf(fmaxf(s0[0], s0[1]), fmaxf(s0[2], s0[3])), fmaxf(fmaxf(s1[0], s1[1]), fmaxf(s1[2], s1[3])));
      m = max32x(max16x(m));
      if (g == 0) LD(float, L_WM + 4 * (r * 4 + v)) = m;
      signal(4); waitc(4, 4);
      const f32x4 wm = LD(f32x4, L_WM + 16 * r);
      m = fmaxf(fmaxf(wm[0], wm[1]), fmaxf(wm[2], wm[3]));
      float l = 0;
      #pragma unroll
      for (int e = 0; e < 4; ++e) { s0[e] = __builtin_amdgcn_exp2f(s0[e] - m); s1[e] = __builtin_amdgcn_exp2f(s1[e] - m); l += s0[e] + s1[e]; }
      LD(u32x2, L_P + r * 256 + (((4 * v + (g >> 1)) ^ fz) << 4) + 8 * (g & 1)) = u32x2{pkbf(s0[0], s0[1]), pkbf(s0[2], s0[3])};
      LD(u32x2, L_P + r * 256 + (((4 * v + 2 + (g >> 1)) ^ fz) << 4) + 8 * (g & 1)) = u32x2{pkbf(s1[0], s1[1]), pkbf(s1[2], s1[3])};
      l = sum32x(sum16x(l));
      if (g == 0) LD(float, L_WL + 4 * (r * 4 + v)) = l;
      signal(5);
    }
    waitc(5, 4);
    STAMP(7);
    if (v == 7 && lane < 16) { const f32x4 wm = LD(f32x4, L_WM + 16 * lane), wl = LD(f32x4, L_WL + 16 * lane);
      u32x4 gr = {asu(fmaxf(fmaxf(wm[0], wm[1]), fmaxf(wm[2], wm[3]))), TL, asu(wl[0] + wl[1] + wl[2] + wl[3]), TH};
      GSTC(WS_ST + ((ba * 32 + 16 * hh + lane) * 64 + kt) * 16, gr, ws); }
    { f32x4 o[4] = {{0, 0, 0, 0}, {0, 0, 0, 0}, {0, 0, 0, 0}, {0, 0, 0, 0}};
      const u32 fz = swz(r), q4 = r >> 2, p4 = r & 3;
      #pragma unroll
      for (int t = 0; t < 4; ++t) {
        const u32x4 bp = LD(u32x4, L_P + r * 256 + (((4 * t + g) ^ fz) << 4));
        const u32 k0 = 32 * t + 8 * g + q4, k1 = k0 + 4;
        #pragma unroll
        for (int rb = 0; rb < 4; ++rb) { const u32 ch = 8 * v + 2 * rb + (p4 >> 1);
          const s16x4 a0 = __builtin_amdgcn_ds_read_tr16_b64_v4i16((LDSP(s16x4))(sm + L_CKV + k0 * 1024 + ((ch ^ swz(k0 & 15)) << 4) + 8 * (p4 & 1)));
          const s16x4 a1 = __builtin_amdgcn_ds_read_tr16_b64_v4i16((LDSP(s16x4))(sm + L_CKV + k1 * 1024 + ((ch ^ swz(k1 & 15)) << 4) + 8 * (p4 & 1)));
          const u32x2 b0 = __builtin_bit_cast(u32x2, a0), b1 = __builtin_bit_cast(u32x2, a1);
          o[rb] = mfma(u32x4{b0[0], b0[1], b1[0], b1[1]}, bp, o[rb]); }
      }
      #pragma unroll
      for (int rb = 0; rb < 4; ++rb) { u32x4 gr = {pkbf(o[rb][0], o[rb][1]), TL, pkbf(o[rb][2], o[rb][3]), TH};
        const u32 c0 = 64 * v + 16 * rb;
        GSTC(WS_PT + ((((ba * 32 + 16 * hh + r) * 4 + (c0 >> 7)) * 64 + kt) * 32 + ((c0 & 127) >> 2) + g) * 16, gr, ws); }
    }
    signal(7);
    STAMP(8);
    { u32x4 st, pt[4];
      const u32 qh = v & 1, sg = v >> 1;
      const u32 os = WS_ST + ((b * 32 + h) * 64 + lane) * 16;
      const u32 op = WS_PT + ((((b * 32 + h) * 4 + j) * 64 + 16 * sg + g) * 32 + 16 * qh + r) * 16;
      _Pragma("nounroll") for (int it = 0; it < SPIN_MAX; ++it) { POLL1(st, os, ws); if (__all(TAGOK(st))) break; }
      #pragma unroll
      for (int k = 0; k < 4; ++k) GLDC(pt[k], op + k * 2048, ws);
      const float M = wmax(asf(st[0]));
      const u32 ab = pkbf(__builtin_amdgcn_exp2f(asf(st[0]) - M), 0.f) & 0xFFFFu;
      const float Linv = 1.f / wsum(asf(ab << 16) * asf(st[2]));
      const u32 adup = ab | (ab << 16);
      u32 al[4];
      #pragma unroll
      for (int k = 0; k < 4; ++k) al[k] = __builtin_amdgcn_ds_bpermute(4 * (16 * sg + 4 * k + g), adup);
      WAIT4(0, pt[0], pt[1], pt[2], pt[3]);
      if (!__all(TAGOK(pt[0]) & TAGOK(pt[1]) & TAGOK(pt[2]) & TAGOK(pt[3]))) {
        _Pragma("nounroll") for (int it = 0; it < SPIN_MAX; ++it) {
          POLL4(pt[0], pt[1], pt[2], pt[3], op, ws, 2048, 4096, 6144);
          if (__all(TAGOK(pt[0]) & TAGOK(pt[1]) & TAGOK(pt[2]) & TAGOK(pt[3]))) break; } }
      STAMP(9);
      const u32 m0 = r == 0 ? 0xFFFFu : r == 1 ? 0xFFFF0000u : 0u, m2 = r == 2 ? 0xFFFFu : r == 3 ? 0xFFFF0000u : 0u;
      f32x4 a = {0, 0, 0, 0};
      #pragma unroll
      for (int k = 0; k < 4; ++k) a = mfma(pt[k], u32x4{al[k] & m0, 0u, al[k] & m2, 0u}, a);
      waitc(7, 8);
      if (r < 4) {
        #pragma unroll
        for (int e = 0; e < 4; ++e) LD(float, L_CPART + 16 * (4 * (16 * qh + 4 * g + e) + r) + 4 * sg) = a[e] * Linv; }
    }
    BAR();
    BAR();
    if (tid < 128) { const f32x4 p0 = LD(f32x4, L_VPART + 32 * tid), p1 = LD(f32x4, L_VPART + 32 * tid + 16);
      const float s = (p0[0] + p0[1]) + (p0[2] + p0[3]) + (p1[0] + p1[1]) + (p1[2] + p1[3]);
      LD(u32, L_CD + 4 * ((tid >> 6) * 64 + (tid & 3) * 16 + ((tid >> 2) & 15))) = pkbf(s, s); }
    BAR();
    BAR();
    if (tid < 256) { const f32x4 p = LD(f32x4, L_OPART + 16 * tid);
      u32x4 gr = {asu(p[0] + p[1]), TL, asu(p[2] + p[3]), TH};
      GSTC(WS_OP + (((b * 32 + (tid >> 3)) * 128 + prod) * 8 + (tid & 7)) * 16, gr, ws); }
    STAMP(10);
  } else {
    const u32 v = wid - 8;
    const u32 fb = wg >> 3, ks = wg & 7;
    const bool f1 = (wg < 176) && (v < 4);
    u32x4 f1r[4];
    if (f1) { const u32 col = min(64 * fb + 8 * (r & 7), 1376u - 8);
      const u32 o = (64 * ks + 16 * v + g) * 2752 + col * 2;
      const char* wsel = (r >> 3) ? w3 : w1;
      #pragma unroll
      for (int k = 0; k < 4; ++k) asm volatile("global_load_dwordx4 %0, %1, off" : "=v"(f1r[k]) : "v"(wsel + o + k * 4 * 2752)); }
    { const u32 o = (ba * 8192 + kt * 128 + 16 * v) * 1024;
      #pragma unroll
      for (int k = 0; k < 16; ++k) GLDS(o + k * 1024 + ((lane ^ swz(k)) << 4), ckv, smb + L_CKV + (16 * v + k) * 1024); }
    STAMP(1);
    GET_TAGS();
    if (f1) {
      waitc(0, 2);
      const u32x4 xd = LD(u32x4, L_XD + (r >> 3) * 2048 + 4 * (ks * 64 + g * 16 + 4 * v));
      WAIT4(16, f1r[0], f1r[1], f1r[2], f1r[3]);
      f32x4 a = {0, 0, 0, 0};
      #pragma unroll
      for (int k = 0; k < 4; ++k) a = mfma(f1r[k], DB(xd[k]), a);
      #pragma unroll
      for (int e = 0; e < 4; ++e) { const u32 row = 4 * g + e;
        LD(float, L_F1 + v * 1024 + 4 * ((((r >> 3) * 64 + (row & 7) * 8 + (r & 7)) << 1) + (row >> 3))) = a[e]; }
      signal(6);
      if (v < 2) { waitc(6, 4);
        f32x2 s = LD(f32x2, L_F1 + 8 * (v * 64 + lane));
        #pragma unroll
        for (int k = 1; k < 4; ++k) s += LD(f32x2, L_F1 + k * 1024 + 8 * (v * 64 + lane));
        u32x4 gr = {asu(s[0]), TL, asu(s[1]), TH};
        if (64 * fb + lane < 1376) GSTC(WS_FP + (((v * 22 + fb) * 8 + ks) * 64 + lane) * 16, gr, ws); }
    }
    WAITV(0);
    signal(3);
    STAMP(2);
    const bool f2 = prod < 86;
    if (v == 7) {
      float hv = 0.f;
      if (f2) { u32x4 q0, q1; const u32 f0 = 16 * prod;
        const u32 o = WS_FP + (((b * 22 + (f0 >> 6)) * 8 + g) * 64 + (f0 & 63) + r) * 16;
        _Pragma("nounroll") for (int it = 0; it < SPIN_MAX; ++it) { POLL2(q0, q1, o, o + 4096, ws);
          if (__all(TAGOK(q0) & TAGOK(q1))) break; }
        const float gg = sum32x(sum16x(asf(q0[0]) + asf(q1[0]))), uu = sum32x(sum16x(asf(q0[2]) + asf(q1[2])));
        hv = gg / (1.f + __expf(-gg)) * uu; }
      if (lane < 16) LD(u32, L_HD + 4 * lane) = pkbf(hv, hv);
    }
    u32x4 uvr[4], w2r[2], wor[16];
    const u32 ns = v & 3, kh = v >> 2;
    { const u32 o = (128 * j + 16 * v + g) * 8192 + h * 256 + r * 16;
      #pragma unroll
      for (int k = 0; k < 4; ++k) GLD(uvr[k], o + k * 32768, wuv); }
    { const u32 o = (min(16 * prod, 1376u - 16) + 8 * kh + g) * 1024 + ns * 256 + r * 16;
      GLD(w2r[0], o, w2); GLD(w2r[1], o + 4096, w2); }
    { const u32 o = (h * 128 + 64 * kh + g) * 1024 + ns * 256 + r * 16;
      #pragma unroll
      for (int k = 0; k < 16; ++k) GLD(wor[(k + 0) & 15], o + k * 4096, wo); }
    STAMP(3);
    BAR();
    STAMP(4);
    { WAIT4(18, uvr[0], uvr[1], uvr[2], uvr[3]);
      f32x4 a = {0, 0, 0, 0};
      #pragma unroll
      for (int k = 0; k < 4; ++k) { const f32x4 c = LD(f32x4, L_CPART + 16 * (16 * v + 4 * k + g)); const float s = (c[0] + c[1]) + (c[2] + c[3]);
        a = mfma(uvr[k], DB(pkbf(s, s)), a); }
      const u32 d0 = 32 * g + 16 * (r >> 3) + (r & 7);
      LD(float, L_VPART + 32 * d0 + 4 * v) = (r < 8) ? a[0] : a[2];
      LD(float, L_VPART + 32 * (d0 + 8) + 4 * v) = (r < 8) ? a[1] : a[3];
    }
    BAR();
    BAR();
    { u32x4 cd[4];
      #pragma unroll
      for (int k = 0; k < 4; ++k) cd[k] = LD(u32x4, L_CD + 4 * (kh * 64 + g * 16 + 4 * k));
      const u32 h0 = LD(u32, L_HD + 4 * (8 * kh + g)), h1 = LD(u32, L_HD + 4 * (8 * kh + 4 + g));
      WAIT2(0, w2r[0], w2r[1]);
      #pragma unroll
      for (int k = 0; k < 16; k += 4) WAIT4(0, wor[k], wor[k + 1], wor[k + 2], wor[k + 3]);
      f32x4 a0 = {0, 0, 0, 0}, a1 = {0, 0, 0, 0};
      #pragma unroll
      for (int k = 0; k < 16; k += 2) { a0 = mfma(wor[k], DB(cd[k >> 2][k & 3]), a0); a1 = mfma(wor[k + 1], DB(cd[k >> 2][(k & 3) + 1]), a1); }
      if (f2) { a0 = mfma(w2r[0], DB(h0), a0); a1 = mfma(w2r[1], DB(h1), a1); }
      a0 += a1;
      const u32 n0 = 128 * ns + 32 * g + 16 * (r >> 3) + (r & 7);
      LD(float, L_OPART + 8 * n0 + 4 * kh) = (r < 8) ? a0[0] : a0[2];
      LD(float, L_OPART + 8 * (n0 + 8) + 4 * kh) = (r < 8) ? a0[1] : a0[3];
    }
    BAR();
    STAMP(5);
  }
  if (j == 0) {
    u32x4 q, q2; const u32 o = WS_OP + (((b * 32 + h) * 128) * 8 + tid) * 16;
    u32 xv = 0; if (tid < 16) xv = *(const u16*)(x + b * 1024 + (16 * h + tid) * 2);
    const bool last = (wg == 0) && (wid < 2);
    _Pragma("nounroll") for (int it = 0; it < SPIN_MAX; ++it) { POLL1(q, o, ws); if (__all(TAGOK(q))) break; }
    float a = asf(q[0]), c = asf(q[2]);
    a += dppf<0x128>(a); c += dppf<0x128>(c);
    a = sum32x(sum16x(a)); c = sum32x(sum16x(c));
    if (lane < 8) LD(f32x2, L_FIN + wid * 64 + 8 * lane) = f32x2{a, c};
    if (last) { const u32 o2 = WS_OP + ((32 * 128 + wid * 64 + lane) * 8) * 16;
      _Pragma("nounroll") for (int it = 0; it < SPIN_MAX; ++it) { POLL1(q2, o2, ws); if (__all(TAGOK(q2))) break; } }
    BAR();
    if (tid < 16) { float s = asf(xv << 16);
      #pragma unroll
      for (int k = 0; k < 16; ++k) s += LD(float, L_FIN + k * 64 + 4 * tid);
      out[b * 512 + 16 * h + tid] = (u16)(pkbf(s, s) & 0xFFFFu); }
    if (wg == 0 && tid == 0) *(u64*)ws = E + 1;
  }
  if (wg == 0 && tid == 0) { for (int it = 0; it < 200000; ++it) { if (__builtin_amdgcn_s_memrealtime() - tstart >= 4000) break; } }
  STAMP(11);
}

extern "C" void kernel_launch(void* const* d_in, const int* in_sizes, int n_in, void* d_out, int out_size, void* d_ws, size_t ws_size, hipStream_t stream) {
  mla_ffn_fused<<<256, 1024, 0, stream>>>((const char*)d_in[0], (const char*)d_in[1], (const char*)d_in[2], (const char*)d_in[3], (const char*)d_in[4],
      (const char*)d_in[5], (const char*)d_in[7], (const char*)d_in[8], (const char*)d_in[9], (u16*)d_out, (char*)d_ws);
}
```

```cpp
#include <hip/hip_runtime.h>
typedef unsigned u32; typedef unsigned long long u64; typedef unsigned short u16;
typedef u32 u32x4 __attribute__((ext_vector_type(4)));
typedef u32 u32x2 __attribute__((ext_vector_type(2)));
typedef float f32x4 __attribute__((ext_vector_type(4)));
typedef float f32x2 __attribute__((ext_vector_type(2)));
typedef __bf16 bf16x8 __attribute__((ext_vector_type(8)));
typedef short s16x4 __attribute__((ext_vector_type(4)));
#define LDSP(T) __attribute__((address_space(3))) T*
constexpr u32 WS_QA = 4096;
constexpr u32 WS_QP = WS_QA + 131072;
constexpr u32 WS_FP = WS_QP + 262144;
constexpr u32 WS_ST = WS_FP + 180224;
constexpr u32 WS_OP = WS_ST + 65536;
constexpr u32 OP_STR = 16384 + 1152, PT_STR = 32768 + 4608;
constexpr u32 WS_PT = WS_OP + 64 * OP_STR;
constexpr u32 L_END = 163840;
constexpr u32 L_CKV = 0, L_Q = 131072, L_WM = L_Q, L_WL = L_Q + 1024, L_P = L_Q + 2048, L_FIN = 0;
constexpr u32 L_XD = 0  , L_QPART = 4096  , L_QV = 20480  , L_UV = 32768  , L_UK = 102400  , L_F1 = 65536  ,
              L_HD = 81920  , L_CP = 82176  , L_VD = 84224  , L_OPART = 86016  ;
constexpr int SPIN_MAX = 20000;
#define ADELAY 0

__device__ __forceinline__ f32x4 mfma(u32x4 a, u32x4 b, f32x4 c) {
  return __builtin_amdgcn_mfma_f32_16x16x32_bf16(__builtin_bit_cast(bf16x8, a), __builtin_bit_cast(bf16x8, b), c, 0, 0, 0); }
typedef __bf16 bf16x2 __attribute__((ext_vector_type(2)));
__device__ __forceinline__ u32 pkbf(float a, float b) { return __builtin_bit_cast(u32, __builtin_convertvector(f32x2{a, b}, bf16x2)); }
__device__ __forceinline__ float asf(u32 v) { return __builtin_bit_cast(float, v); }
__device__ __forceinline__ u32 asu(float v) { return __builtin_bit_cast(u32, v); }
#define GLD(dst, voff, sbase) asm volatile("global_load_dwordx4 %0, %1, %2" : "=v"(dst) : "v"(voff), "s"(sbase))
#define GLDC(dst, voff, sbase) asm volatile("global_load_dwordx4 %0, %1, %2 sc1" : "=v"(dst) : "v"(voff), "s"(sbase))
#define GSTC(voff, data, sbase) asm volatile("global_store_dwordx4 %0, %1, %2 sc1\n\ts_nop 2" :: "v"(voff), "v"(data), "s"(sbase) : "memory")
#define GLDS(voff, sbase, ldsaddr) asm volatile("s_mov_b32 m0, %2\n\ts_nop 0\n\tglobal_load_lds_dwordx4 %0, %1" :: "v"(voff), "s"(sbase), "s"(ldsaddr) : "memory")
#define WAITV(n) asm volatile("s_waitcnt vmcnt(%0)" :: "n"(n) : "memory")
#define TIE(r) asm volatile("" : "+v"(r))
#define SB() __builtin_amdgcn_sched_barrier(0)
#define WAIT4(n, a, b, c, d) do { asm volatile("s_waitcnt vmcnt(%4)" : "+v"(a), "+v"(b), "+v"(c), "+v"(d) : "n"(n) : "memory"); } while (0)
#define WAIT2(n, a, b) do { asm volatile("s_waitcnt vmcnt(%2)" : "+v"(a), "+v"(b) : "n"(n) : "memory"); } while (0)
#define WAIT1(n, a) do { asm volatile("s_waitcnt vmcnt(%1)" : "+v"(a) : "n"(n) : "memory"); } while (0)
#define POLL1(a, o0, sb) asm volatile("global_load_dwordx4 %0, %1, %2 sc1\n\ts_waitcnt vmcnt(0)" : "=&v"(a) : "v"(o0), "s"(sb) : "memory")
#define POLL2(a, b, o0, o1, sb) asm volatile("global_load_dwordx4 %0, %2, %4 sc1\n\tglobal_load_dwordx4 %1, %3, %4 sc1\n\ts_waitcnt vmcnt(0)" : "=&v"(a), "=&v"(b) : "v"(o0), "v"(o1), "s"(sb) : "memory")
#define POLL4(a, b, c, d, o0, sb, i1, i2, i3) asm volatile("global_load_dwordx4 %0, %4, %8 sc1\n\tglobal_load_dwordx4 %1, %5, %8 sc1\n\tglobal_load_dwordx4 %2, %6, %8 sc1\n\tglobal_load_dwordx4 %3, %7, %8 sc1\n\ts_waitcnt vmcnt(0)" \
   : "=&v"(a), "=&v"(b), "=&v"(c), "=&v"(d) : "v"(o0), "v"((o0) + (i1)), "v"((o0) + (i2)), "v"((o0) + (i3)), "s"(sb) : "memory")
#define TAGOK(q) (((q)[1] == TL) & ((q)[3] == TH))
#define BAR() asm volatile("s_waitcnt lgkmcnt(0)\n\ts_barrier" ::: "memory")
template <int CTRL> __device__ __forceinline__ float dppf(float v) { return asf(__builtin_amdgcn_update_dpp(0, asu(v), CTRL, 0xf, 0xf, true)); }
__device__ __forceinline__ float sum16x(float v) { auto r = __builtin_amdgcn_permlane16_swap(asu(v), asu(v), false, false); return asf(r[0]) + asf(r[1]); }
__device__ __forceinline__ float sum32x(float v) { auto r = __builtin_amdgcn_permlane32_swap(asu(v), asu(v), false, false); return asf(r[0]) + asf(r[1]); }
__device__ __forceinline__ float max16x(float v) { auto r = __builtin_amdgcn_permlane16_swap(asu(v), asu(v), false, false); return fmaxf(asf(r[0]), asf(r[1])); }
__device__ __forceinline__ float max32x(float v) { auto r = __builtin_amdgcn_permlane32_swap(asu(v), asu(v), false, false); return fmaxf(asf(r[0]), asf(r[1])); }
__device__ __forceinline__ float wsum(float v) { v += dppf<0xB1>(v); v += dppf<0x4E>(v); v += dppf<0x141>(v); v += dppf<0x140>(v); v = sum16x(v); return sum32x(v); }
__device__ __forceinline__ float wmax(float v) { v = fmaxf(v, dppf<0xB1>(v)); v = fmaxf(v, dppf<0x4E>(v)); v = fmaxf(v, dppf<0x141>(v)); v = fmaxf(v, dppf<0x140>(v)); v = max16x(v); return max32x(v); }
__device__ __forceinline__ u32 swz(u32 r) { return ((r & 3) << 2) | (((r >> 3) & 1) << 1); }

__global__ __launch_bounds__(1024) void mla_ffn_fused(const char* __restrict__ x, const char* __restrict__ ckv, const char* __restrict__ wq,
    const char* __restrict__ wuk, const char* __restrict__ wuv, const char* __restrict__ wo, const char* __restrict__ w1,
    const char* __restrict__ w3, const char* __restrict__ w2, u16* __restrict__ out, char* __restrict__ ws) {
  __shared__ __attribute__((aligned(1024))) char smem[L_END];
  LDSP(char) sm = (LDSP(char))smem;
#define LD(T, off) (*(LDSP(T))(sm + (off)))
  const u32 tid = threadIdx.x, lane = tid & 63;
  const u32 w = __builtin_amdgcn_readfirstlane(tid >> 6);
  const u32 wg = blockIdx.x, xc = wg & 7, ii = wg >> 3;
  const u32 r = lane & 15, g = lane >> 4;
  u64 E0, E; asm volatile("s_load_dwordx2 %0, %1, 0x0" : "=s"(E0) : "s"(ws));
  u32 TL, TH;
#define GET_TAGS() do { asm volatile("s_waitcnt lgkmcnt(0)\n\ts_mov_b64 %0, %1" : "=s"(E) : "s"(E0)); \
  u64 z = E + 0x9E3779B97F4A7C15ull; z = (z ^ (z >> 30)) * 0xBF58476D1CE4E5B9ull; z = (z ^ (z >> 27)) * 0x94D049BB133111EBull; z ^= z >> 31; \
  TL = (u32)z & 0xBFFFBFFFu; TH = (u32)(z >> 32) & 0xBFFFBFFFu; } while (0)

  if (ii < 16) {
    const u32 h = 4 * xc + (ii >> 2), j = ii & 3, wi = 4 * h + j;
    u32x4 mk;
    { const u32 hm = (r & 1) ? 0xFFFF0000u : 0x0000FFFFu, t = (r & 7) >> 1; mk = u32x4{t == 0 ? hm : 0u, t == 1 ? hm : 0u, t == 2 ? hm : 0u, t == 3 ? hm : 0u}; }
#define DB(xd) (u32x4{(xd) & mk[0], (xd) & mk[1], (xd) & mk[2], (xd) & mk[3]})
    const u32 bh = r >> 3, r7 = r & 7;
    u32x4 xr, ukr[2], wqr[2];
    if (w < 2) GLD(xr, w * 1024 + lane * 16, x);
    { const u32 o = (128 * j + 8 * w + g) * 8192 + h * 256 + r * 16;
      GLD(wqr[0], o, wq); GLD(wqr[1], o + 32768, wq); GLD(ukr[0], o, wuk); GLD(ukr[1], o + 32768, wuk); }
    if (w < 2) {
      WAIT1(4, xr);
      float f[8];
      #pragma unroll
      for (int e = 0; e < 4; ++e) { f[2 * e] = asf(xr[e] << 16); f[2 * e + 1] = asf(xr[e] & 0xFFFF0000u); }
      float s = 0; for (int e = 0; e < 8; ++e) s += f[e];
      const float mean = wsum(s) * (1.f / 512);
      float q = 0; for (int e = 0; e < 8; ++e) { f[e] -= mean; q += f[e] * f[e]; }
      const float rstd = 1.f / sqrtf(wsum(q) * (1.f / 512) + 1e-5f);
      #pragma unroll
      for (int e = 0; e < 8; ++e) { const u32 d = pkbf(f[e] * rstd, f[e] * rstd);
        LD(u32, L_XD + w * 2048 + 4 * ((lane >> 2) * 32 + (e & 3) * 8 + 2 * (lane & 3) + (e >> 2))) = d; }
    }
    GET_TAGS();
    BAR();
    {
      const u32x2 xd = LD(u32x2, L_XD + bh * 2048 + 4 * ((4 * j + (w >> 2)) * 32 + g * 8 + 2 * (w & 3)));
      f32x4 a = {0, 0, 0, 0};
      SB(); WAIT2(2, wqr[0], wqr[1]);
      a = mfma(wqr[0], DB(xd[0]), a); a = mfma(wqr[1], DB(xd[1]), a);
      #pragma unroll
      for (int e = 0; e < 4; ++e) LD(float, L_QPART + 4 * (w * 256 + bh * 128 + 32 * g + 8 * e + r7)) = a[e];
    }
    BAR();
    { float s = 0;
      #pragma unroll
      for (int k = 0; k < 4; ++k) s += LD(float, L_QPART + 4 * ((4 * g + k) * 256 + 16 * w + r));
      s = sum32x(sum16x(s));
      const float sn = dppf<0xB1>(s);
      u32x4 gr = {asu(s), TL, asu(sn), TH};
      if (g == 0 && !(r & 1)) GSTC(WS_QP + ((h * 4 + j) * 128 + 8 * w + (r >> 1)) * 16, gr, ws); }
    SB(); WAIT2(0, ukr[0], ukr[1]);
    #pragma unroll
    for (int k = 0; k < 2; ++k) { const u32 row = 8 * w + 4 * k + g; LD(u32x4, L_UK + row * 256 + ((r ^ swz(row & 15)) << 4)) = ukr[k]; }
    if (w < 2) { u32x4 q0, q1, q2, q3;
      const u32 o = WS_QP + (h * 4 * 128 + 64 * w + lane) * 16;
      _Pragma("nounroll") for (int it = 0; it < SPIN_MAX; ++it) { POLL4(q0, q1, q2, q3, o, ws, 2048, 4096, 6144);
        if (__all(TAGOK(q0) & TAGOK(q1) & TAGOK(q2) & TAGOK(q3))) break; }
      LD(u32, L_QV + 256 * w + 4 * lane) = pkbf((asf(q0[0]) + asf(q1[0])) + (asf(q2[0]) + asf(q3[0])), (asf(q0[2]) + asf(q1[2])) + (asf(q2[2]) + asf(q3[2]))); }
    BAR();
    if (w < 8) {
      u32x4 ua[4], qb[4];
      #pragma unroll
      for (int k = 0; k < 4; ++k) { ua[k] = LD(u32x4, L_UK + (16 * w + r) * 256 + (((4 * k + g) ^ swz(r)) << 4)); qb[k] = LD(u32x4, L_QV + (r & 1) * 256 + 64 * k + 16 * g); }
      SB();
      f32x4 a = {0, 0, 0, 0};
      #pragma unroll
      for (int k = 0; k < 4; ++k) a = mfma(ua[k], qb[k], a);
      const float sc = 0.08838834764831845f * 1.4426950408889634f;
      u32x4 gr = {pkbf(a[0] * sc, a[1] * sc), TL, pkbf(a[2] * sc, a[3] * sc), TH};
      if (r < 2) GSTC(WS_QA + ((r * 32 + h) * 128 + 32 * j + 4 * w + g) * 16, gr, ws);
    }
    BAR();
    u32x4 f1r[2], uvr[2], w2r, wor[8];
    const u32 fb = min(wi >> 2, 21u), ks = wi & 3, ns = w & 3, kq = w >> 2;
    { const u32 col = min(64 * fb + 8 * r7, 1376u - 8);
      const u32 o = (128 * ks + 8 * w + g) * 2752 + col * 2;
      const char* wsel = bh ? w3 : w1;
      #pragma unroll
      for (int k = 0; k < 2; ++k) asm volatile("global_load_dwordx4 %0, %1, off" : "=v"(f1r[k]) : "v"(wsel + o + k * 4 * 2752)); }
    { const u32 o = (128 * j + 8 * w + g) * 8192 + h * 256 + r * 16;
      GLD(uvr[0], o, wuv); GLD(uvr[1], o + 32768, wuv); }
    GLD(w2r, (min(16 * wi, 1376u - 16) + 4 * kq + g) * 1024 + ns * 256 + r * 16, w2);
    { const u32 o = (h * 128 + 32 * kq + g) * 1024 + ns * 256 + r * 16;
      #pragma unroll
      for (int k = 0; k < 8; ++k) GLD(wor[k], o + k * 4096, wo); }
    { const u32x2 xd = LD(u32x2, L_XD + bh * 2048 + 4 * ((4 * ks + (w >> 2)) * 32 + g * 8 + 2 * (w & 3)));
      SB(); WAIT2(11, f1r[0], f1r[1]);
      f32x4 a = {0, 0, 0, 0};
      a = mfma(f1r[0], DB(xd[0]), a); a = mfma(f1r[1], DB(xd[1]), a);
      #pragma unroll
      for (int e = 0; e < 4; ++e) { const u32 row = 4 * g + e;
        LD(float, L_F1 + w * 1024 + 4 * (((bh * 64 + (row & 7) * 8 + r7) << 1) + (row >> 3))) = a[e]; }
      SB(); WAIT2(9, uvr[0], uvr[1]);
      #pragma unroll
      for (int k = 0; k < 2; ++k) { const u32 row = 8 * w + 4 * k + g; LD(u32x4, L_UV + row * 256 + ((r ^ swz(row & 15)) << 4)) = uvr[k]; }
    }
    BAR();
    if (w < 2) {
      f32x2 s = LD(f32x2, L_F1 + 8 * (w * 64 + lane));
      #pragma unroll
      for (int k = 1; k < 16; ++k) s += LD(f32x2, L_F1 + k * 1024 + 8 * (w * 64 + lane));
      u32x4 gr = {asu(s[0]), TL, asu(s[1]), TH};
      if (wi < 88 && 64 * fb + lane < 1376) GSTC(WS_FP + (((w * 22 + fb) * 4 + ks) * 64 + lane) * 16, gr, ws);
    }
    if (w == 2) {
      float h0 = 0.f, h1 = 0.f;
      if (wi < 86) { u32x4 q0, q1; const u32 f0 = 16 * wi;
        const u32 o = WS_FP + (((f0 >> 6) * 4 + g) * 64 + (f0 & 63) + r) * 16;
        _Pragma("nounroll") for (int it = 0; it < SPIN_MAX; ++it) { POLL2(q0, q1, o, o + 22 * 4 * 64 * 16, ws);
          if (__all(TAGOK(q0) & TAGOK(q1))) break; __builtin_amdgcn_s_sleep(4); }
        const float g0 = sum32x(sum16x(asf(q0[0]))), u0 = sum32x(sum16x(asf(q0[2]))), g1 = sum32x(sum16x(asf(q1[0]))), u1 = sum32x(sum16x(asf(q1[2])));
        h0 = g0 / (1.f + __expf(-g0)) * u0; h1 = g1 / (1.f + __expf(-g1)) * u1; }
      if (lane < 16) { LD(u32, L_HD + 4 * lane) = pkbf(h0, h0); LD(u32, L_HD + 64 + 4 * lane) = pkbf(h1, h1); }
    }
    if (w >= 12) {
      const u32 cb = w & 1, ch = (w >> 1) & 1;
      u32x4 st, pt[8];
      const u32 os = WS_ST + ((cb * 32 + h) * 64 + lane) * 16;
      const u32 op = WS_PT + ((cb * 16 + (h >> 1)) * 4 + j) * PT_STR + (32 * ch + g) * 512 + (r >> 2) * 128 + (h & 1) * 64 + (r & 3) * 16;
      _Pragma("nounroll") for (int it = 0; it < SPIN_MAX; ++it) { POLL1(st, os, ws); if (__all(TAGOK(st))) break; __builtin_amdgcn_s_sleep(2); }
      #pragma unroll
      for (int k = 0; k < 8; ++k) GLDC(pt[k], op + k * 2048, ws);
      const float M = wmax(asf(st[0]));
      const u32 ab = pkbf(__builtin_amdgcn_exp2f(asf(st[0]) - M), 0.f) & 0xFFFFu;
      const float Linv = 1.f / wsum(asf(ab << 16) * asf(st[2]));
      const u32 adup = ab | (ab << 16);
      u32 al[8];
      #pragma unroll
      for (int k = 0; k < 8; ++k) al[k] = __builtin_amdgcn_ds_bpermute(4 * (32 * ch + 4 * k + g), adup);
      f32x4 a0 = {0, 0, 0, 0}, a1 = {0, 0, 0, 0};
      SB(); WAIT4(4, pt[0], pt[1], pt[2], pt[3]);
      a0 = mfma(pt[0], DB(al[0]), a0); a1 = mfma(pt[1], DB(al[1]), a1); a0 = mfma(pt[2], DB(al[2]), a0); a1 = mfma(pt[3], DB(al[3]), a1);
      SB(); WAIT4(0, pt[4], pt[5], pt[6], pt[7]);
      a0 = mfma(pt[4], DB(al[4]), a0); a1 = mfma(pt[5], DB(al[5]), a1); a0 = mfma(pt[6], DB(al[6]), a0); a1 = mfma(pt[7], DB(al[7]), a1);
      a0 += a1;
      if (r < 8) {
        #pragma unroll
        for (int e = 0; e < 4; ++e) LD(float, L_CP + 4 * ((ch * 2 + cb) * 128 + 32 * g + 16 * (r >> 2) + 4 * e + (r & 3))) = a0[e] * Linv; }
    }
    BAR();
    if (w < 8) {
      const u32 q4 = r >> 2, p4 = r & 3, chk = 2 * w + (p4 >> 1);
      f32x4 c0[4], c1[4], c2[4], c3[4]; s16x4 t0[4], t1[4];
      #pragma unroll
      for (int t = 0; t < 4; ++t) {
        const u32 cb = L_CP + 4 * ((r & 1) * 128 + 32 * t + 8 * g);
        c0[t] = LD(f32x4, cb); c1[t] = LD(f32x4, cb + 1024); c2[t] = LD(f32x4, cb + 16); c3[t] = LD(f32x4, cb + 1040);
        const u32 k0 = 32 * t + 8 * g + q4, k1 = k0 + 4;
        t0[t] = __builtin_amdgcn_ds_read_tr16_b64_v4i16((LDSP(s16x4))(sm + L_UV + k0 * 256 + ((chk ^ swz(k0 & 15)) << 4) + 8 * (p4 & 1)));
        t1[t] = __builtin_amdgcn_ds_read_tr16_b64_v4i16((LDSP(s16x4))(sm + L_UV + k1 * 256 + ((chk ^ swz(k1 & 15)) << 4) + 8 * (p4 & 1)));
      }
      SB();
      f32x4 a = {0, 0, 0, 0}, a2 = {0, 0, 0, 0};
      #pragma unroll
      for (int t = 0; t < 4; ++t) {
        const u32x2 b0 = __builtin_bit_cast(u32x2, t0[t]), b1 = __builtin_bit_cast(u32x2, t1[t]);
        const f32x4 x0 = c0[t] + c1[t], x1 = c2[t] + c3[t];
        const u32x4 A = {b0[0], b0[1], b1[0], b1[1]}, B = {pkbf(x0[0], x0[1]), pkbf(x0[2], x0[3]), pkbf(x1[0], x1[1]), pkbf(x1[2], x1[3])};
        if (t & 1) a2 = mfma(A, B, a2); else a = mfma(A, B, a);
      }
      a += a2;
      if (r < 2) {
        #pragma unroll
        for (int e = 0; e < 4; ++e) LD(u32, L_VD + r * 512 + 4 * ((w >> 1) * 32 + e * 8 + ((4 * w + g) & 7))) = pkbf(a[e], a[e]); }
    }
    BAR();
    { const u32x4 v0 = LD(u32x4, L_VD + bh * 512 + 4 * (kq * 32 + g * 8)), v1 = LD(u32x4, L_VD + bh * 512 + 4 * (kq * 32 + g * 8 + 4));
      const u32 hd = LD(u32, L_HD + bh * 64 + 4 * (4 * kq + g));
      WAIT1(0, w2r); WAIT4(0, wor[0], wor[1], wor[2], wor[3]); WAIT4(0, wor[4], wor[5], wor[6], wor[7]);
      f32x4 a0 = {0, 0, 0, 0}, a1 = {0, 0, 0, 0};
      #pragma unroll
      for (int k = 0; k < 4; ++k) { a0 = mfma(wor[k], DB(v0[k]), a0); a1 = mfma(wor[4 + k], DB(v1[k]), a1); }
      if (wi < 86) a0 = mfma(w2r, DB(hd), a0);
      a0 += a1;
      #pragma unroll
      for (int e = 0; e < 4; ++e) LD(float, L_OPART + 16 * (bh * 512 + 128 * ns + 32 * g + 8 * e + r7) + 4 * kq) = a0[e];
    }
    BAR();
    if (tid < 512) { const f32x4 p0 = LD(f32x4, L_OPART + 32 * tid), p1 = LD(f32x4, L_OPART + 32 * tid + 16);
      u32x4 gr = {asu((p0[0] + p0[1]) + (p0[2] + p0[3])), TL, asu((p1[0] + p1[1]) + (p1[2] + p1[3])), TH};
      GSTC(WS_OP + (tid >> 3) * OP_STR + (wi * 8 + (tid & 7)) * 16, gr, ws); }
  } else {
    const u32 T = xc * 16 + (ii - 16), ba = T >> 6, kt = T & 63;
    const u32 fz = swz(r), k8 = w & 7, ht = w >> 3;
    if (ADELAY) __builtin_amdgcn_s_sleep(ADELAY);
    { u32x4 cr[8];
      const u32 o = (ba * 8192 + kt * 128 + 8 * w) * 1024 + lane * 16;
      #pragma unroll
      for (int k = 0; k < 8; ++k) GLD(cr[k], o + k * 1024, ckv);
      GET_TAGS();
      const u32 lo = L_CKV + 8 * w * 1024, lx = lane ^ ((w & 1) << 1);
      #pragma unroll
      for (int k = 0; k < 8; k += 4) {
        SB(); WAIT4(4 - k, cr[k], cr[k + 1], cr[k + 2], cr[k + 3]);
        #pragma unroll
        for (int e = 0; e < 4; ++e) LD(u32x4, lo + (k + e) * 1024 + ((lx ^ (e << 2)) << 4)) = cr[k + e]; }
    }
    BAR();
    u32x4 af[16];
    #pragma unroll
    for (int t = 0; t < 16; ++t) af[t] = LD(u32x4, L_CKV + (16 * k8 + r) * 1024 + (((4 * t + g) ^ fz) << 4));
    { u32x4 q[4];
      const u32 o = WS_QA + ((ba * 32 + 2 * w) * 128 + lane) * 16;
      { const u32 oc = WS_QA + ((ba * 32 + 2 * w + ((lane >> 2) & 1)) * 128 + 32 * (lane & 3) + 31) * 16;
        _Pragma("nounroll") for (int it = 0; it < SPIN_MAX; ++it) { bool ok = true; if (lane < 8) { POLL1(q[0], oc, ws); ok = TAGOK(q[0]); }
          if (__all(ok)) break; __builtin_amdgcn_s_sleep(2); } }
      _Pragma("nounroll") for (int it = 0; it < SPIN_MAX; ++it) {
        POLL4(q[0], q[1], q[2], q[3], o, ws, 1024, 2048, 3072);
        if (__all(TAGOK(q[0]) & TAGOK(q[1]) & TAGOK(q[2]) & TAGOK(q[3]))) break;
      }
      #pragma unroll
      for (int k = 0; k < 4; ++k) { const u32 hd = 2 * w + (k >> 1), qd = 64 * (k & 1) + lane;
        LD(u32x2, L_Q + hd * 1024 + (((qd >> 1) ^ swz(hd & 15)) << 4) + 8 * (qd & 1)) = u32x2{q[k][0], q[k][2]}; }
    }
    BAR();
    f32x4 s0 = {0, 0, 0, 0}, s1 = {0, 0, 0, 0};
    { u32x4 bq[8];
      #pragma unroll
      for (int t = 0; t < 8; ++t) bq[t] = LD(u32x4, L_Q + (16 * ht + r) * 1024 + (((4 * t + g) ^ fz) << 4));
      SB();
      #pragma unroll
      for (int t = 0; t < 8; ++t) { if (t & 1) s1 = mfma(af[t], bq[t], s1); else s0 = mfma(af[t], bq[t], s0);
        bq[t] = LD(u32x4, L_Q + (16 * ht + r) * 1024 + (((4 * t + 32 + g) ^ fz) << 4)); }
      SB();
      #pragma unroll
      for (int t = 0; t < 8; ++t) { if (t & 1) s1 = mfma(af[8 + t], bq[t], s1); else s0 = mfma(af[8 + t], bq[t], s0); }
    }
    s0 += s1;
    float m = max32x(max16x(fmaxf(fmaxf(s0[0], s0[1]), fmaxf(s0[2], s0[3]))));
    BAR();
    if (g == 0) LD(float, L_WM + 32 * (16 * ht + r) + 4 * k8) = m;
    BAR();
    { const f32x4 m0 = LD(f32x4, L_WM + 32 * (16 * ht + r)), m1 = LD(f32x4, L_WM + 32 * (16 * ht + r) + 16);
      m = fmaxf(fmaxf(fmaxf(m0[0], m0[1]), fmaxf(m0[2], m0[3])), fmaxf(fmaxf(m1[0], m1[1]), fmaxf(m1[2], m1[3]))); }
    float l = 0;
    #pragma unroll
    for (int e = 0; e < 4; ++e) { s0[e] = __builtin_amdgcn_exp2f(s0[e] - m); l += s0[e]; }
    LD(u32x2, L_P + (16 * ht + r) * 256 + (((2 * k8 + (g >> 1)) ^ fz) << 4) + 8 * (g & 1)) = u32x2{pkbf(s0[0], s0[1]), pkbf(s0[2], s0[3])};
    l = sum32x(sum16x(l));
    if (g == 0) LD(float, L_WL + 32 * (16 * ht + r) + 4 * k8) = l;
    BAR();
    { f32x4 o[2][2] = {{{0, 0, 0, 0}, {0, 0, 0, 0}}, {{0, 0, 0, 0}, {0, 0, 0, 0}}};
      const u32 q4 = r >> 2, p4 = r & 3;
      u32x4 bp0[4], bp1[4]; s16x4 ta[4][2][2];
      #pragma unroll
      for (int t = 0; t < 4; ++t) {
        bp0[t] = LD(u32x4, L_P + r * 256 + (((4 * t + g) ^ fz) << 4)); bp1[t] = LD(u32x4, L_P + (16 + r) * 256 + (((4 * t + g) ^ fz) << 4));
        const u32 k0 = 32 * t + 8 * g + q4, k1 = k0 + 4;
        #pragma unroll
        for (int ct = 0; ct < 2; ++ct) { const u32 chk = 4 * w + 2 * ct + (p4 >> 1);
          ta[t][ct][0] = __builtin_amdgcn_ds_read_tr16_b64_v4i16((LDSP(s16x4))(sm + L_CKV + k0 * 1024 + ((chk ^ swz(k0 & 15)) << 4) + 8 * (p4 & 1)));
          ta[t][ct][1] = __builtin_amdgcn_ds_read_tr16_b64_v4i16((LDSP(s16x4))(sm + L_CKV + k1 * 1024 + ((chk ^ swz(k1 & 15)) << 4) + 8 * (p4 & 1))); }
      }
      SB();
      #pragma unroll
      for (int t = 0; t < 4; ++t) {
        #pragma unroll
        for (int ct = 0; ct < 2; ++ct) {
          const u32x2 b0 = __builtin_bit_cast(u32x2, ta[t][ct][0]), b1 = __builtin_bit_cast(u32x2, ta[t][ct][1]);
          const u32x4 a = {b0[0], b0[1], b1[0], b1[1]};
          o[ct][0] = mfma(a, bp0[t], o[ct][0]); o[ct][1] = mfma(a, bp1[t], o[ct][1]); }
      }
      #pragma unroll
      for (int hx = 0; hx < 2; ++hx) {
        u32x4 d = {pkbf(o[0][hx][0], o[0][hx][1]), pkbf(o[0][hx][2], o[0][hx][3]), pkbf(o[1][hx][0], o[1][hx][1]), pkbf(o[1][hx][2], o[1][hx][3])};
        GSTC(WS_PT + ((ba * 16 + 8 * hx + (r >> 1)) * 4 + (w >> 2)) * PT_STR + kt * 512 + (w & 3) * 128 + (r & 1) * 64 + g * 16, d, ws); }
    }
    u32x4 sg = {0, TL, 0, TH};
    if (w == 0 && lane < 32) { const f32x4 m0 = LD(f32x4, L_WM + 32 * lane), m1 = LD(f32x4, L_WM + 32 * lane + 16), l0 = LD(f32x4, L_WL + 32 * lane), l1 = LD(f32x4, L_WL + 32 * lane + 16);
      sg[0] = asu(fmaxf(fmaxf(fmaxf(m0[0], m0[1]), fmaxf(m0[2], m0[3])), fmaxf(fmaxf(m1[0], m1[1]), fmaxf(m1[2], m1[3]))));
      sg[2] = asu(((l0[0] + l0[1]) + (l0[2] + l0[3])) + ((l1[0] + l1[1]) + (l1[2] + l1[3]))); }
    WAITV(0);
    BAR();
    if (w == 0 && lane < 32) GSTC(WS_ST + ((ba * 32 + lane) * 64 + kt) * 16, sg, ws);
    if ((T & 1) == 0) {
      const u32 cg = (T >> 1) & 31;
      u32x4 q; const u32 o = WS_OP + (ba * 32 + cg) * OP_STR + tid * 16;
      u32 xv = 0; if (tid < 16) xv = *(const u16*)(x + ba * 1024 + (16 * cg + tid) * 2);
      _Pragma("nounroll") for (int it = 0; it < SPIN_MAX; ++it) { POLL1(q, o, ws); if (__all(TAGOK(q))) break; __builtin_amdgcn_s_sleep(4); }
      float a = asf(q[0]), c = asf(q[2]);
      a += dppf<0x128>(a); c += dppf<0x128>(c);
      a = sum32x(sum16x(a)); c = sum32x(sum16x(c));
      if (lane < 8) LD(f32x2, L_FIN + w * 64 + 8 * lane) = f32x2{a, c};
      BAR();
      if (tid < 16) { float s = asf(xv << 16);
        #pragma unroll
        for (int k = 0; k < 16; ++k) s += LD(float, L_FIN + k * 64 + 4 * tid);
        out[ba * 512 + 16 * cg + tid] = (u16)(pkbf(s, s) & 0xFFFFu); }
      if (T == 0 && tid == 0) *(u64*)ws = E + 1;
    }
  }
}

extern "C" void kernel_launch(void* const* d_in, const int* in_sizes, int n_in, void* d_out, int out_size, void* d_ws, size_t ws_size, hipStream_t stream) {
  mla_ffn_fused<<<256, 1024, 0, stream>>>((const char*)d_in[0], (const char*)d_in[1], (const char*)d_in[2], (const char*)d_in[3], (const char*)d_in[4],
      (const char*)d_in[5], (const char*)d_in[7], (const char*)d_in[8], (const char*)d_in[9], (u16*)d_out, (char*)d_ws);
}
```

```cpp
#include <hip/hip_runtime.h>
typedef unsigned u32; typedef unsigned long long u64; typedef unsigned short u16;
typedef u32 u32x4 __attribute__((ext_vector_type(4)));
typedef u32 u32x2 __attribute__((ext_vector_type(2)));
typedef float f32x4 __attribute__((ext_vector_type(4)));
typedef float f32x2 __attribute__((ext_vector_type(2)));
typedef __bf16 bf16x8 __attribute__((ext_vector_type(8)));
typedef short s16x4 __attribute__((ext_vector_type(4)));
#define LDSP(T) __attribute__((address_space(3))) T*
constexpr u32 WS_QA = 4096;
constexpr u32 WS_QP = WS_QA + 131072;
constexpr u32 WS_FP = WS_QP + 262144;
constexpr u32 WS_ST = WS_FP + 180224;
constexpr u32 WS_OP = WS_ST + 65536;
constexpr u32 OP_STR = 16384 + 1152, PT_STR = 32768 + 4608;
constexpr u32 WS_PT = WS_OP + 64 * OP_STR;
constexpr u32 L_END = 163840;
constexpr u32 L_CKV = 0, L_Q = 131072, L_WM = L_Q, L_P = L_Q + 2048, L_FIN = L_Q + 16384;
constexpr u32 L_XD = 0  , L_QPART = 4096  , L_QV = 20480  , L_UV = 32768  , L_UK = 102400  , L_F1 = 65536  ,
              L_HD = 81920  , L_CP = 82176  , L_VD = 84224  , L_OPART = 86016  , L_STG = 102400  , L_OP2 = L_F1;
constexpr int SPIN_MAX = 20000;
#define RSLEEP 2
#define ADELAY 0

__device__ __forceinline__ f32x4 mfma(u32x4 a, u32x4 b, f32x4 c) {
  return __builtin_amdgcn_mfma_f32_16x16x32_bf16(__builtin_bit_cast(bf16x8, a), __builtin_bit_cast(bf16x8, b), c, 0, 0, 0); }
typedef __bf16 bf16x2 __attribute__((ext_vector_type(2)));
__device__ __forceinline__ u32 pkbf(float a, float b) { return __builtin_bit_cast(u32, __builtin_convertvector(f32x2{a, b}, bf16x2)); }
__device__ __forceinline__ float asf(u32 v) { return __builtin_bit_cast(float, v); }
__device__ __forceinline__ u32 asu(float v) { return __builtin_bit_cast(u32, v); }
#define GLD(dst, voff, sbase) asm volatile("global_load_dwordx4 %0, %1, %2" : "=v"(dst) : "v"(voff), "s"(sbase))
#define GLDC(dst, voff, sbase) asm volatile("global_load_dwordx4 %0, %1, %2 sc1" : "=v"(dst) : "v"(voff), "s"(sbase))
#define GSTC(voff, data, sbase) asm volatile("global_store_dwordx4 %0, %1, %2 sc1\n\ts_nop 2" :: "v"(voff), "v"(data), "s"(sbase) : "memory")
#define GLDS(voff, sbase, ldsaddr) asm volatile("s_mov_b32 m0, %2\n\ts_nop 0\n\tglobal_load_lds_dwordx4 %0, %1" :: "v"(voff), "s"(sbase), "s"(ldsaddr) : "memory")
#define WAITV(n) asm volatile("s_waitcnt vmcnt(%0)" :: "n"(n) : "memory")
#define TIE(r) asm volatile("" : "+v"(r))
#define PIN(x) asm volatile("" : "+v"(x))
#define SB() __builtin_amdgcn_sched_barrier(0)
#define WAIT4(n, a, b, c, d) do { asm volatile("s_waitcnt vmcnt(%4)" : "+v"(a), "+v"(b), "+v"(c), "+v"(d) : "n"(n) : "memory"); } while (0)
#define WAIT2(n, a, b) do { asm volatile("s_waitcnt vmcnt(%2)" : "+v"(a), "+v"(b) : "n"(n) : "memory"); } while (0)
#define WAIT1(n, a) do { asm volatile("s_waitcnt vmcnt(%1)" : "+v"(a) : "n"(n) : "memory"); } while (0)
#define POLL1(a, o0, sb) asm volatile("global_load_dwordx4 %0, %1, %2 sc1\n\ts_waitcnt vmcnt(0)" : "=&v"(a) : "v"(o0), "s"(sb) : "memory")
#define POLL2(a, b, o0, o1, sb) asm volatile("global_load_dwordx4 %0, %2, %4 sc1\n\tglobal_load_dwordx4 %1, %3, %4 sc1\n\ts_waitcnt vmcnt(0)" : "=&v"(a), "=&v"(b) : "v"(o0), "v"(o1), "s"(sb) : "memory")
#define POLL4(a, b, c, d, o0, sb, i1, i2, i3) asm volatile("global_load_dwordx4 %0, %4, %8 sc1\n\tglobal_load_dwordx4 %1, %5, %8 sc1\n\tglobal_load_dwordx4 %2, %6, %8 sc1\n\tglobal_load_dwordx4 %3, %7, %8 sc1\n\ts_waitcnt vmcnt(0)" \
   : "=&v"(a), "=&v"(b), "=&v"(c), "=&v"(d) : "v"(o0), "v"((o0) + (i1)), "v"((o0) + (i2)), "v"((o0) + (i3)), "s"(sb) : "memory")
#define TAGOK(q) (((q)[1] == TL) & ((q)[3] == TH))
#define BAR() asm volatile("s_waitcnt lgkmcnt(0)\n\ts_barrier" ::: "memory")
template <int CTRL> __device__ __forceinline__ float dppf(float v) { return asf(__builtin_amdgcn_update_dpp(0, asu(v), CTRL, 0xf, 0xf, true)); }
__device__ __forceinline__ float sum16x(float v) { auto r = __builtin_amdgcn_permlane16_swap(asu(v), asu(v), false, false); return asf(r[0]) + asf(r[1]); }
__device__ __forceinline__ float sum32x(float v) { auto r = __builtin_amdgcn_permlane32_swap(asu(v), asu(v), false, false); return asf(r[0]) + asf(r[1]); }
__device__ __forceinline__ float max16x(float v) { auto r = __builtin_amdgcn_permlane16_swap(asu(v), asu(v), false, false); return fmaxf(asf(r[0]), asf(r[1])); }
__device__ __forceinline__ float max32x(float v) { auto r = __builtin_amdgcn_permlane32_swap(asu(v), asu(v), false, false); return fmaxf(asf(r[0]), asf(r[1])); }
__device__ __forceinline__ float max3(float a, float b, float c) { float d; asm("v_max3_f32 %0, %1, %2, %3" : "=v"(d) : "v"(a), "v"(b), "v"(c)); return d; }
__device__ __forceinline__ float max2(float a, float b) { float d; asm("v_max_f32 %0, %1, %2" : "=v"(d) : "v"(a), "v"(b)); return d; }
__device__ __forceinline__ u32 umax3(u32 a, u32 b, u32 c) { u32 d; asm("v_max3_u32 %0, %1, %2, %3" : "=v"(d) : "v"(a), "v"(b), "v"(c)); return d; }
__device__ __forceinline__ float wsum(float v) { v += dppf<0xB1>(v); v += dppf<0x4E>(v); v += dppf<0x141>(v); v += dppf<0x140>(v); v = sum16x(v); return sum32x(v); }
__device__ __forceinline__ float wmax(float v) { v = fmaxf(v, dppf<0xB1>(v)); v = fmaxf(v, dppf<0x4E>(v)); v = fmaxf(v, dppf<0x141>(v)); v = fmaxf(v, dppf<0x140>(v)); v = max16x(v); return max32x(v); }
__device__ __forceinline__ u32 swz(u32 r) { return ((r & 3) << 2) | (((r >> 3) & 1) << 1); }

__global__ __launch_bounds__(1024) void mla_ffn_fused(const char* __restrict__ x, const char* __restrict__ ckv, const char* __restrict__ wq,
    const char* __restrict__ wuk, const char* __restrict__ wuv, const char* __restrict__ wo, const char* __restrict__ w1,
    const char* __restrict__ w3, const char* __restrict__ w2, u16* __restrict__ out, char* __restrict__ ws) {
  __shared__ __attribute__((aligned(1024))) char smem[L_END];
  LDSP(char) sm = (LDSP(char))smem;
#define LD(T, off) (*(LDSP(T))(sm + (off)))
  const u32 tid = threadIdx.x, lane = tid & 63;
  const u32 w = __builtin_amdgcn_readfirstlane(tid >> 6);
  const u32 wg = blockIdx.x, xc = wg & 7, ii = wg >> 3;
  const u32 r = lane & 15, g = lane >> 4;
  u64 E0, E; asm volatile("s_load_dwordx2 %0, %1, 0x0" : "=s"(E0) : "s"(ws));
  u32 TL, TH;
#define GET_TAGS() do { asm volatile("s_waitcnt lgkmcnt(0)\n\ts_mov_b64 %0, %1" : "=s"(E) : "s"(E0)); \
  u64 z = E + 0x9E3779B97F4A7C15ull; z = (z ^ (z >> 30)) * 0xBF58476D1CE4E5B9ull; z = (z ^ (z >> 27)) * 0x94D049BB133111EBull; z ^= z >> 31; \
  TL = (u32)z & 0xBFFFBFFFu; TH = (u32)(z >> 32) & 0xBFFFBFFFu; } while (0)

  if (ii < 16) {
    const u32 h = 4 * xc + (ii >> 2), j = ii & 3, wi = 4 * h + j;
    u32x4 mk;
    { const u32 hm = (r & 1) ? 0xFFFF0000u : 0x0000FFFFu, t = (r & 7) >> 1; mk = u32x4{t == 0 ? hm : 0u, t == 1 ? hm : 0u, t == 2 ? hm : 0u, t == 3 ? hm : 0u}; }
#define DB(xd) (u32x4{(xd) & mk[0], (xd) & mk[1], (xd) & mk[2], (xd) & mk[3]})
    const u32 bh = r >> 3, r7 = r & 7;
    u32x4 xr, ukr[2], wqr[2];
    if (w < 2) GLD(xr, w * 1024 + lane * 16, x);
    u32 owq = (128 * j + 8 * w + g) * 8192 + h * 256 + r * 16;
    GLD(wqr[0], owq, wq); GLD(wqr[1], owq + 32768, wq);
    if (w < 2) {
      WAIT1(2, xr);
      float f[8];
      #pragma unroll
      for (int e = 0; e < 4; ++e) { f[2 * e] = asf(xr[e] << 16); f[2 * e + 1] = asf(xr[e] & 0xFFFF0000u); }
      float s = 0; for (int e = 0; e < 8; ++e) s += f[e];
      const float mean = wsum(s) * (1.f / 512);
      float q = 0; for (int e = 0; e < 8; ++e) { f[e] -= mean; q += f[e] * f[e]; }
      const float rstd = 1.f / sqrtf(wsum(q) * (1.f / 512) + 1e-5f);
      #pragma unroll
      for (int e = 0; e < 8; ++e) { const u32 d = pkbf(f[e] * rstd, f[e] * rstd);
        LD(u32, L_XD + w * 2048 + 4 * ((lane >> 2) * 32 + (e & 3) * 8 + 2 * (lane & 3) + (e >> 2))) = d; }
    }
    GET_TAGS();
    BAR();
    GLD(ukr[0], owq, wuk); GLD(ukr[1], owq + 32768, wuk);
    {
      const u32x2 xd = LD(u32x2, L_XD + bh * 2048 + 4 * ((4 * j + (w >> 2)) * 32 + g * 8 + 2 * (w & 3)));
      f32x4 a = {0, 0, 0, 0};
      SB(); WAIT2(2, wqr[0], wqr[1]);
      a = mfma(wqr[0], DB(xd[0]), a); a = mfma(wqr[1], DB(xd[1]), a);
      #pragma unroll
      for (int e = 0; e < 4; ++e) LD(float, L_QPART + 4 * (w * 256 + bh * 128 + 32 * g + 8 * e + r7)) = a[e];
    }
    BAR();
    { float s = 0;
      #pragma unroll
      for (int k = 0; k < 4; ++k) s += LD(float, L_QPART + 4 * ((4 * g + k) * 256 + 16 * w + r));
      s = sum32x(sum16x(s));
      const float sn = dppf<0xB1>(s);
      u32x4 gr = {asu(s), TL, asu(sn), TH};
      if (g == 0 && !(r & 1)) GSTC(WS_QP + ((h * 4 + j) * 128 + 8 * w + (r >> 1)) * 16, gr, ws); }
    SB(); WAIT2(0, ukr[0], ukr[1]);
    #pragma unroll
    for (int k = 0; k < 2; ++k) { const u32 row = 8 * w + 4 * k + g; LD(u32x4, L_UK + row * 256 + ((r ^ swz(row & 15)) << 4)) = ukr[k]; }
    if (w < 2) { u32x4 q0, q1, q2, q3;
      const u32 o = WS_QP + (h * 4 * 128 + 64 * w + lane) * 16;
      _Pragma("nounroll") for (int it = 0; it < SPIN_MAX; ++it) { POLL4(q0, q1, q2, q3, o, ws, 2048, 4096, 6144);
        if (__all(TAGOK(q0) & TAGOK(q1) & TAGOK(q2) & TAGOK(q3))) break; }
      LD(u32, L_QV + 256 * w + 4 * lane) = pkbf((asf(q0[0]) + asf(q1[0])) + (asf(q2[0]) + asf(q3[0])), (asf(q0[2]) + asf(q1[2])) + (asf(q2[2]) + asf(q3[2]))); }
    BAR();
    if (w < 8) {
      u32x4 ua[4], qb[4];
      #pragma unroll
      for (int k = 0; k < 4; ++k) { ua[k] = LD(u32x4, L_UK + (16 * w + r) * 256 + (((4 * k + g) ^ swz(r)) << 4)); qb[k] = LD(u32x4, L_QV + (r & 1) * 256 + 64 * k + 16 * g); }
      SB();
      f32x4 a = {0, 0, 0, 0};
      #pragma unroll
      for (int k = 0; k < 4; ++k) a = mfma(ua[k], qb[k], a);
      const float sc = 0.08838834764831845f * 1.4426950408889634f;
      u32x4 gr = {pkbf(a[0] * sc, a[1] * sc), TL, pkbf(a[2] * sc, a[3] * sc), TH};
      if (r < 2) GSTC(WS_QA + ((r * 32 + h) * 128 + 32 * j + 4 * w + g) * 16, gr, ws);
    }
    BAR();
    u32x4 f1r[2], uvr[2], w2r, wor[8];
    const u32 fb = min(wi >> 2, 21u), ks = wi & 3, ns = w & 3, kq = w >> 2;
    { const u32 col = min(64 * fb + 8 * r7, 1376u - 8);
      const u32 o = (128 * ks + 8 * w + g) * 2752 + col * 2;
      const char* wsel = bh ? w3 : w1;
      #pragma unroll
      for (int k = 0; k < 2; ++k) asm volatile("global_load_dwordx4 %0, %1, off" : "=v"(f1r[k]) : "v"(wsel + o + k * 4 * 2752)); }
    { const u32 o = (128 * j + 8 * w + g) * 8192 + h * 256 + r * 16;
      GLD(uvr[0], o, wuv); GLD(uvr[1], o + 32768, wuv); }
    asm volatile("s_barrier" ::: "memory");
    GLD(w2r, (min(16 * wi, 1376u - 16) + 4 * kq + g) * 1024 + ns * 256 + r * 16, w2);
    u32 owo = (h * 128 + 32 * kq + g) * 1024 + ns * 256 + r * 16;
    #pragma unroll
    for (int k = 0; k < 2; ++k) GLD(wor[k], owo + ((k + 2 * j) & 7) * 4096, wo);
    { const u32x2 xd = LD(u32x2, L_XD + bh * 2048 + 4 * ((4 * ks + (w >> 2)) * 32 + g * 8 + 2 * (w & 3)));
      SB(); WAIT2(5, f1r[0], f1r[1]);
      f32x4 a = {0, 0, 0, 0};
      a = mfma(f1r[0], DB(xd[0]), a); a = mfma(f1r[1], DB(xd[1]), a);
      #pragma unroll
      for (int e = 0; e < 4; ++e) { const u32 row = 4 * g + e;
        LD(float, L_F1 + w * 1024 + 4 * (((bh * 64 + (row & 7) * 8 + r7) << 1) + (row >> 3))) = a[e]; }
      SB(); WAIT2(3, uvr[0], uvr[1]);
      #pragma unroll
      for (int k = 0; k < 2; ++k) { const u32 row = 8 * w + 4 * k + g; LD(u32x4, L_UV + row * 256 + ((r ^ swz(row & 15)) << 4)) = uvr[k]; }
    }
    BAR();
    if ((w >> 1) == 6) {
      f32x2 s = LD(f32x2, L_F1 + 8 * ((w & 1) * 64 + lane));
      #pragma unroll
      for (int k = 1; k < 16; ++k) s += LD(f32x2, L_F1 + k * 1024 + 8 * ((w & 1) * 64 + lane));
      u32x4 gr = {asu(s[0]), TL, asu(s[1]), TH};
      if (wi < 88 && 64 * fb + lane < 1376) GSTC(WS_FP + ((((w & 1) * 22 + fb) * 4 + ks) * 64 + lane) * 16, gr, ws);
    }
    asm volatile("s_barrier" ::: "memory");
    #pragma unroll
    for (int k = 2; k < 8; ++k) GLD(wor[k], owo + ((k + 2 * j) & 7) * 4096, wo);
    const u32 q4 = r >> 2, p4 = r & 3;
    u32x4 wof[8], uvf[4];
    { WAIT1(0, w2r); WAIT4(0, wor[0], wor[1], wor[2], wor[3]); WAIT4(0, wor[4], wor[5], wor[6], wor[7]);
      const u32 sb = w == 15 ? L_QPART : L_STG + 4096 * w;
      const u32 sw0 = sb + g * 256 + ((r ^ (g << 1)) << 4), sw1 = sb + (4 + g) * 256 + ((r ^ ((4 + g) << 1)) << 4);
      const u32 row = 4 * g + q4, sr = sb + row * 256 + (((p4 >> 1) ^ ((row & 7) << 1)) << 4) + 8 * (p4 & 1);
      #pragma unroll
      for (int hf = 0; hf < 2; ++hf) {
        LD(u32x4, sw0) = wor[4 * hf]; LD(u32x4, sw1) = wor[4 * hf + 1]; LD(u32x4, sw0 + 2048) = wor[4 * hf + 2]; LD(u32x4, sw1 + 2048) = wor[4 * hf + 3];
        asm volatile("" ::: "memory");
        #pragma unroll
        for (int i = 0; i < 8; ++i) { const u32x2 t = __builtin_bit_cast(u32x2, __builtin_amdgcn_ds_read_tr16_b64_v4i16((LDSP(s16x4))(sm + (sr ^ (i << 5)))));
          wof[i][2 * hf] = t[0]; wof[i][2 * hf + 1] = t[1]; }
        asm volatile("s_waitcnt lgkmcnt(0)" ::: "memory");
      }
    }
    if (w == 14) {
      float h0 = 0.f, h1 = 0.f;
      if (wi < 86) { u32x4 q0, q1; const u32 f0 = 16 * wi;
        const u32 o = WS_FP + (((f0 >> 6) * 4 + g) * 64 + (f0 & 63) + r) * 16;
        _Pragma("nounroll") for (int it = 0; it < SPIN_MAX; ++it) { POLL2(q0, q1, o, o + 22 * 4 * 64 * 16, ws);
          if (__all(TAGOK(q0) & TAGOK(q1))) break; __builtin_amdgcn_s_sleep(4); }
        const float g0 = sum32x(sum16x(asf(q0[0]))), u0 = sum32x(sum16x(asf(q0[2]))), g1 = sum32x(sum16x(asf(q1[0]))), u1 = sum32x(sum16x(asf(q1[2])));
        h0 = g0 / (1.f + __expf(-g0)) * u0; h1 = g1 / (1.f + __expf(-g1)) * u1; }
      if (lane < 16) { LD(u32, L_HD + 4 * lane) = pkbf(h0, h0); LD(u32, L_HD + 64 + 4 * lane) = pkbf(h1, h1); }
    }
    const u32 vi = (4 * (w & 1) + g - 2 * j) & 7, vw = (w - 4) & 7;
    u32 a_cp = L_CP + (r & 1) * 256 + 16 * g, a_vw = L_VD + (r & 1) * 256 + (vw >> 1) * 64 + (vi & 3) * 16 + (vi >> 2) * 8, a_vr = L_VD + (r & 1) * 256 + kq * 64 + g * 16,
        a_ow = L_OPART + (kq * 2 + (r & 1)) * 2112 + 4 * (128 * ns + 4 * g), a_or = L_OPART + ((tid >> 8) & 1) * 2112 + 8 * (tid & 255),
        a_op = WS_OP + (tid >> 3) * OP_STR + (wi * 8 + (tid & 7)) * 16,
        a_hd = L_HD + bh * 64 + 4 * (4 * kq + g), a_o2 = L_OP2 + 16 * (bh * 512 + 128 * ns + 32 * g + r7) + 4 * kq;
    PIN(a_cp); PIN(a_vw); PIN(a_vr); PIN(a_ow); PIN(a_or); PIN(a_op); PIN(a_hd); PIN(a_o2);
    if (w < 4) {
      const u32 cb = w & 1, ch = (w >> 1) & 1;
      u32x4 st, pt[8], al[8];
      const u32 os = WS_ST + ((cb * 64 + lane) * 32 + h) * 16;
      u32 op = WS_PT + ((cb * 16 + (h >> 1)) * 4 + j) * PT_STR + (32 * ch + g) * 512 + (r >> 2) * 128 + (h & 1) * 64 + (r & 3) * 16;
      u32 a_cw = L_CP + (ch * 2 + cb) * 256 + 2 * (32 * g + 16 * ((r >> 2) & 1) + (r & 3));
      PIN(op); PIN(a_cw);
      _Pragma("nounroll") for (int it = 0; it < SPIN_MAX; ++it) { bool ok = true; if (lane == ((5 * wi + 16 * w) & 63)) { POLL1(st, os, ws); ok = TAGOK(st); }
        if (__all(ok)) break; __builtin_amdgcn_s_sleep(RSLEEP); }
      _Pragma("nounroll") for (int it = 0; it < SPIN_MAX; ++it) { POLL1(st, os, ws); if (__all(TAGOK(st))) break; __builtin_amdgcn_s_sleep(2); }
#define PTLD() asm volatile("global_load_dwordx4 %0, %8, %10 sc1\n\tglobal_load_dwordx4 %1, %8, %10 offset:2048 sc1\n\t" \
      "global_load_dwordx4 %2, %9, %10 sc1\n\tglobal_load_dwordx4 %3, %9, %10 offset:2048 sc1\n\t" \
      "v_add_u32 %8, 0x2000, %8\n\tv_add_u32 %9, 0x2000, %9\n\t" \
      "global_load_dwordx4 %4, %8, %10 sc1\n\tglobal_load_dwordx4 %5, %8, %10 offset:2048 sc1\n\t" \
      "global_load_dwordx4 %6, %9, %10 sc1\n\tglobal_load_dwordx4 %7, %9, %10 offset:2048 sc1" \
      : "=&v"(pt[0]), "=&v"(pt[1]), "=&v"(pt[2]), "=&v"(pt[3]), "=&v"(pt[4]), "=&v"(pt[5]), "=&v"(pt[6]), "=&v"(pt[7]), "+v"(o0), "+v"(o1) : "s"(ws) : "memory")
#define PTWAIT() asm volatile("s_waitcnt vmcnt(0)" : "+v"(pt[0]), "+v"(pt[1]), "+v"(pt[2]), "+v"(pt[3]), "+v"(pt[4]), "+v"(pt[5]), "+v"(pt[6]), "+v"(pt[7]) :: "memory")
      { u32 o0 = op, o1 = op + 4096; PTLD(); }
      const float M = wmax(asf(st[0]));
      const u32 ab = pkbf(__builtin_amdgcn_exp2f(asf(st[0]) - M), 0.f) & 0xFFFFu;
      float Linv = 1.f / wsum(asf(ab << 16) * asf(st[2]));
      const u32 adup = ab | (ab << 16);
      #pragma unroll
      for (int k = 0; k < 8; ++k) { const u32 av = __builtin_amdgcn_ds_bpermute(4 * (32 * ch + 4 * k + g), adup); al[k] = DB(av); PIN(al[k]); }
      PIN(Linv);
      _Pragma("nounroll") for (int it = 0; it < SPIN_MAX; ++it) {
        PTWAIT();
        u32 mx = umax3(pt[0][1], pt[0][3], 0);
        #pragma unroll
        for (int k = 1; k < 8; ++k) mx = umax3(pt[k][1], pt[k][3], mx);
        if (__all(mx != 0xFFFFFFFFu)) break;
        u32 o0 = op, o1 = op + 4096; PTLD();
      }
      PTWAIT();
      f32x4 a0 = {0, 0, 0, 0}, a1 = {0, 0, 0, 0};
      #pragma unroll
      for (int k = 0; k < 8; k += 2) { a0 = mfma(pt[k], al[k], a0); a1 = mfma(pt[k + 1], al[k + 1], a1); }
      a0 = (a0 + a1) * Linv;
      const u32 p01 = pkbf(a0[0], a0[1]), p23 = pkbf(a0[2], a0[3]);
      if (r < 8) { LD(u16, a_cw) = (u16)p01; LD(u16, a_cw + 8) = (u16)(p01 >> 16); LD(u16, a_cw + 16) = (u16)p23; LD(u16, a_cw + 24) = (u16)(p23 >> 16); }
    } else if (w < 12) {
      const u32 chk = 2 * vw + (p4 >> 1), k0 = 8 * g + q4, ua = L_UV + k0 * 256 + ((chk ^ swz(k0 & 15)) << 4) + 8 * (p4 & 1);
      #pragma unroll
      for (int t = 0; t < 4; ++t) {
        const u32x2 b0 = __builtin_bit_cast(u32x2, __builtin_amdgcn_ds_read_tr16_b64_v4i16((LDSP(s16x4))(sm + ua + t * 8192)));
        const u32x2 b1 = __builtin_bit_cast(u32x2, __builtin_amdgcn_ds_read_tr16_b64_v4i16((LDSP(s16x4))(sm + ua + t * 8192 + 1024)));
        uvf[t] = u32x4{b0[0], b0[1], b1[0], b1[1]}; }
      PIN(uvf[0]); PIN(uvf[1]); PIN(uvf[2]); PIN(uvf[3]);
    }
    BAR();
    if (w >= 4 && w < 12) {
      u32x4 c[8];
      #pragma unroll
      for (int t = 0; t < 8; ++t) c[t] = LD(u32x4, a_cp + (t >> 2) * 512 + (t & 3) * 64);
      SB();
      f32x4 a = {0, 0, 0, 0}, a2 = {0, 0, 0, 0};
      #pragma unroll
      for (int t = 0; t < 4; ++t) { a = mfma(uvf[t], c[t], a); a2 = mfma(uvf[t], c[4 + t], a2); }
      a += a2;
      if (r < 2) LD(u32x2, a_vw) = u32x2{pkbf(a[0], a[1]), pkbf(a[2], a[3])};
    }
    { const u32 hd = LD(u32, a_hd);
      f32x4 a0 = {0, 0, 0, 0};
      if (wi < 86) a0 = mfma(w2r, DB(hd), a0);
      #pragma unroll
      for (int e = 0; e < 4; ++e) LD(float, a_o2 + 128 * e) = a0[e];
    }
    BAR();
    { const u32x4 v = LD(u32x4, a_vr);
      f32x4 o[8];
      #pragma unroll
      for (int i = 0; i < 8; ++i) o[i] = mfma(wof[i], v, f32x4{0, 0, 0, 0});
      if (r < 2) {
        #pragma unroll
        for (int i = 0; i < 8; ++i) LD(f32x4, a_ow + 64 * i) = o[i]; }
    }
    BAR();
    if (tid < 512) {
      const f32x4 p0 = LD(f32x4, L_OP2 + 32 * tid), p1 = LD(f32x4, L_OP2 + 32 * tid + 16);
      const f32x2 yff = {(p0[0] + p0[1]) + (p0[2] + p0[3]), (p1[0] + p1[1]) + (p1[2] + p1[3])};
      const f32x2 s = ((LD(f32x2, a_or) + LD(f32x2, a_or + 4224)) + (LD(f32x2, a_or + 8448) + LD(f32x2, a_or + 12672))) + yff;
      u32x4 gr = {asu(s[0]), TL, asu(s[1]), TH};
      GSTC(a_op, gr, ws); }
  } else {
    const u32 T = xc * 16 + (ii - 16), ba = T >> 6, kt = T & 63;
    const u32 fz = swz(r), k8 = w & 7, ht = w >> 3;
    if (ADELAY) __builtin_amdgcn_s_sleep(ADELAY);
    u32 pto = WS_PT + ((ba * 16 + (r >> 1)) * 4 + (w >> 2)) * PT_STR + kt * 512 + (w & 3) * 128 + (r & 1) * 64 + g * 16;
    const char* ws2 = ws + 32 * PT_STR;
    { u32x4 cr[8];
      const u32 o = (ba * 8192 + kt * 128 + 8 * w) * 1024 + lane * 16;
      #pragma unroll
      for (int k = 0; k < 8; ++k) GLD(cr[k], o + k * 1024, ckv);
      GET_TAGS();
      const u32 lo = L_CKV + 8 * w * 1024, lx = lane ^ ((w & 1) << 1);
      #pragma unroll
      for (int k = 0; k < 8; k += 4) {
        SB(); WAIT4(4 - k, cr[k], cr[k + 1], cr[k + 2], cr[k + 3]);
        #pragma unroll
        for (int e = 0; e < 4; ++e) LD(u32x4, lo + (k + e) * 1024 + ((lx ^ (e << 2)) << 4)) = cr[k + e]; }
    }
    BAR();
    u32x4 af[16];
    #pragma unroll
    for (int t = 0; t < 16; ++t) af[t] = LD(u32x4, L_CKV + (16 * k8 + r) * 1024 + (((4 * t + g) ^ fz) << 4));
    const u32 q4 = r >> 2, p4 = r & 3;
    u32 a_qw0 = L_Q + (2 * w) * 1024 + (((lane >> 1) ^ swz((2 * w) & 15)) << 4) + 8 * (lane & 1),
        a_qw1 = L_Q + (2 * w + 1) * 1024 + (((lane >> 1) ^ swz((2 * w + 1) & 15)) << 4) + 8 * (lane & 1);
    u32 a_bq[4], a_pr[4];
    #pragma unroll
    for (int c = 0; c < 4; ++c) { const u32 xc_ = ((4 * c + g) ^ fz) << 4; a_bq[c] = L_Q + (16 * ht + r) * 1024 + xc_; a_pr[c] = L_P + r * 256 + xc_; PIN(a_bq[c]); PIN(a_pr[c]); }
    u32 a_wm = L_WM + 32 * (16 * ht + r), a_wmw = a_wm + 4 * k8, a_pw = L_P + (16 * ht + r) * 256 + (((2 * k8 + (g >> 1)) ^ fz) << 4) + 8 * (g & 1);
    u32 a_t0 = L_CKV + (8 * g + q4) * 1024 + (((4 * w + (p4 >> 1)) ^ swz((8 * g + q4) & 15)) << 4) + 8 * (p4 & 1), a_t1 = a_t0 ^ 32, a_t2 = a_t0 + 65536, a_t3 = a_t1 + 65536;
    u32 a_st = WS_ST + ((ba * 64 + kt) * 32 + 16 * ht + r) * 16;
    PIN(a_qw0); PIN(a_qw1); PIN(a_wm); PIN(a_wmw); PIN(a_pw); PIN(a_t0); PIN(a_t1); PIN(a_t2); PIN(a_t3); PIN(a_st); PIN(pto);
    { u32x4 q[4];
      const u32 o = WS_QA + ((ba * 32 + 2 * w) * 128 + lane) * 16;
      if (w == 0) {
        const u32 orp = WS_QA + ((ba * 32 + (kt & 31)) * 128 + 32 * ((kt >> 5) + 2 * (xc & 1)) + 31) * 16;
        _Pragma("nounroll") for (int it = 0; it < SPIN_MAX; ++it) { bool ok = true; if (lane == 0) { POLL1(q[0], orp, ws); ok = TAGOK(q[0]); }
          if (__all(ok)) break; __builtin_amdgcn_s_sleep(RSLEEP); } }
      BAR();
      { const u32 oc = WS_QA + ((ba * 32 + 2 * w + ((lane >> 2) & 1)) * 128 + 32 * (lane & 3) + 31) * 16;
        _Pragma("nounroll") for (int it = 0; it < SPIN_MAX; ++it) { bool ok = true; if (lane < 8) { POLL1(q[0], oc, ws); ok = TAGOK(q[0]); }
          if (__all(ok)) break; __builtin_amdgcn_s_sleep(2); } }
      _Pragma("nounroll") for (int it = 0; it < SPIN_MAX; ++it) {
        POLL4(q[0], q[1], q[2], q[3], o, ws, 1024, 2048, 3072);
        if (__all(TAGOK(q[0]) & TAGOK(q[1]) & TAGOK(q[2]) & TAGOK(q[3]))) break;
      }
      { const u32x4 em = {0xFFFFFFFFu, 0xFFFFFFFFu, 0xFFFFFFFFu, 0xFFFFFFFFu}; GSTC(pto, em, ws); GSTC(pto, em, ws2); }
      LD(u32x2, a_qw0) = u32x2{q[0][0], q[0][2]}; LD(u32x2, a_qw0 + 512) = u32x2{q[1][0], q[1][2]};
      LD(u32x2, a_qw1) = u32x2{q[2][0], q[2][2]}; LD(u32x2, a_qw1 + 512) = u32x2{q[3][0], q[3][2]};
    }
    BAR();
    f32x4 s0 = {0, 0, 0, 0}, s1 = {0, 0, 0, 0};
    { u32x4 bq[8];
      #pragma unroll
      for (int t = 0; t < 8; ++t) bq[t] = LD(u32x4, a_bq[t & 3] + (t >> 2) * 256);
      SB();
      #pragma unroll
      for (int t = 0; t < 8; ++t) { if (t & 1) s1 = mfma(af[t], bq[t], s1); else s0 = mfma(af[t], bq[t], s0);
        bq[t] = LD(u32x4, a_bq[t & 3] + (2 + (t >> 2)) * 256); }
      BAR();
      #pragma unroll
      for (int t = 0; t < 8; ++t) { if (t & 1) s1 = mfma(af[8 + t], bq[t], s1); else s0 = mfma(af[8 + t], bq[t], s0); }
    }
    s0 += s1;
    float m = max2(max3(s0[0], s0[1], s0[2]), s0[3]);
    { auto x = __builtin_amdgcn_permlane16_swap(asu(m), asu(m), false, false); m = max2(asf(x[0]), asf(x[1]));
      auto y = __builtin_amdgcn_permlane32_swap(asu(m), asu(m), false, false); m = max2(asf(y[0]), asf(y[1])); }
    LD(float, a_wmw) = m;
    BAR();
    { const f32x4 m0 = LD(f32x4, a_wm), m1 = LD(f32x4, a_wm + 16);
      m = max3(max3(m0[0], m0[1], m0[2]), max3(m0[3], m1[0], m1[1]), max2(m1[2], m1[3])); }
    s0 -= m;
    #pragma unroll
    for (int e = 0; e < 4; ++e) s0[e] = __builtin_amdgcn_exp2f(s0[e]);
    LD(u32x2, a_pw) = u32x2{pkbf(s0[0], s0[1]), pkbf(s0[2], s0[3])};
    WAITV(0);
    BAR();
    { f32x4 o[2][2] = {{{0, 0, 0, 0}, {0, 0, 0, 0}}, {{0, 0, 0, 0}, {0, 0, 0, 0}}};
      u32x4 bp0[4], bp1[4]; s16x4 ta[4][2][2];
      #pragma unroll
      for (int t = 0; t < 4; ++t) { bp0[t] = LD(u32x4, a_pr[t]); bp1[t] = LD(u32x4, a_pr[t] + 4096); }
      #pragma unroll
      for (int t = 0; t < 4; ++t) {
        #pragma unroll
        for (int ct = 0; ct < 2; ++ct) { const u32 a = (t < 2 ? (ct ? a_t1 : a_t0) : (ct ? a_t3 : a_t2)) + (t & 1) * 32768;
          ta[t][ct][0] = __builtin_amdgcn_ds_read_tr16_b64_v4i16((LDSP(s16x4))(sm + a));
          ta[t][ct][1] = __builtin_amdgcn_ds_read_tr16_b64_v4i16((LDSP(s16x4))(sm + a + 4096)); }
      }
      SB();
      if (k8 == 0) {
        const u32x4 one = {0x3F803F80u, 0x3F803F80u, 0x3F803F80u, 0x3F803F80u};
        f32x4 la = {0, 0, 0, 0};
        if (ht == 0) {
          #pragma unroll
          for (int t = 0; t < 4; ++t) la = mfma(one, bp0[t], la);
        } else {
          #pragma unroll
          for (int t = 0; t < 4; ++t) la = mfma(one, bp1[t], la);
        }
        const u32x4 sg = {asu(m), TL, asu(la[0]), TH};
        if (lane < 16) GSTC(a_st, sg, ws);
      }
      SB();
      #pragma unroll
      for (int t = 0; t < 4; ++t) {
        #pragma unroll
        for (int ct = 0; ct < 2; ++ct) {
          const u32x2 b0 = __builtin_bit_cast(u32x2, ta[t][ct][0]), b1 = __builtin_bit_cast(u32x2, ta[t][ct][1]);
          const u32x4 a = {b0[0], b0[1], b1[0], b1[1]};
          o[ct][0] = mfma(a, bp0[t], o[ct][0]); o[ct][1] = mfma(a, bp1[t], o[ct][1]); }
      }
      #pragma unroll
      for (int hx = 0; hx < 2; ++hx) {
        u32x4 d = {pkbf(o[0][hx][0], o[0][hx][1]), pkbf(o[0][hx][2], o[0][hx][3]), pkbf(o[1][hx][0], o[1][hx][1]), pkbf(o[1][hx][2], o[1][hx][3])};
        if (hx == 0) GSTC(pto, d, ws); else GSTC(pto, d, ws2); }
    }
    if ((T & 1) == 0) {
      const u32 cg = (T >> 1) & 31;
      u32x4 q; const u32 o = WS_OP + (ba * 32 + cg) * OP_STR + tid * 16;
      u32 xv = 0; if (tid < 16) xv = *(const u16*)(x + ba * 1024 + (16 * cg + tid) * 2);
      if (w == 0) {
        const u32 orp = WS_OP + (ba * 32 + cg) * OP_STR + ((5 * T + 3) & 127) * 128;
        _Pragma("nounroll") for (int it = 0; it < SPIN_MAX; ++it) { bool ok = true; if (lane == 0) { POLL1(q, orp, ws); ok = TAGOK(q); }
          if (__all(ok)) break; __builtin_amdgcn_s_sleep(RSLEEP); } }
      BAR();
      _Pragma("nounroll") for (int it = 0; it < SPIN_MAX; ++it) { POLL1(q, o, ws); if (__all(TAGOK(q))) break; __builtin_amdgcn_s_sleep(2); }
      float a = asf(q[0]), c = asf(q[2]);
      a += dppf<0x128>(a); c += dppf<0x128>(c);
      a = sum32x(sum16x(a)); c = sum32x(sum16x(c));
      if (lane < 8) LD(f32x2, L_FIN + w * 64 + 8 * lane) = f32x2{a, c};
      BAR();
      if (tid < 16) { float s = asf(xv << 16);
        #pragma unroll
        for (int k = 0; k < 16; ++k) s += LD(float, L_FIN + k * 64 + 4 * tid);
        out[ba * 512 + 16 * cg + tid] = (u16)(pkbf(s, s) & 0xFFFFu); }
      if (T == 0 && tid == 0) *(u64*)ws = E + 1;
    }
  }
}

extern "C" void kernel_launch(void* const* d_in, const int* in_sizes, int n_in, void* d_out, int out_size, void* d_ws, size_t ws_size, hipStream_t stream) {
  mla_ffn_fused<<<256, 1024, 0, stream>>>((const char*)d_in[0], (const char*)d_in[1], (const char*)d_in[2], (const char*)d_in[3], (const char*)d_in[4],
      (const char*)d_in[5], (const char*)d_in[7], (const char*)d_in[8], (const char*)d_in[9], (u16*)d_out, (char*)d_ws);
}
```

```cpp
#define CHEAP 0
#define FINFLAG 1
#define ADELAY 0
#define QA_STR 144
#define CPF 1
#define SKIP 1
#include <hip/hip_runtime.h>
typedef unsigned u32; typedef unsigned long long u64; typedef unsigned short u16;
typedef u32 u32x4 __attribute__((ext_vector_type(4)));
typedef u32 u32x2 __attribute__((ext_vector_type(2)));
typedef float f32x4 __attribute__((ext_vector_type(4)));
typedef float f32x2 __attribute__((ext_vector_type(2)));
typedef __bf16 bf16x8 __attribute__((ext_vector_type(8)));
typedef short s16x4 __attribute__((ext_vector_type(4)));
#define LDSP(T) __attribute__((address_space(3))) T*
constexpr u32 WS_QA = 4096;
constexpr u32 WS_QP = WS_QA + 64 * 16 * QA_STR;
constexpr u32 WS_FP = WS_QP + 262144;
constexpr u32 WS_ST = WS_FP + 180224;
constexpr u32 WS_OP = WS_ST + 65536;
constexpr u32 OP_STR = 16384 + 1152, PT_STR = 32768 + 4608;
constexpr u32 WS_FL = WS_OP + 64 * OP_STR;
constexpr u32 WS_PT = WS_FL + 128 * 128;
constexpr u32 L_END = 163840;
constexpr u32 L_CKV = 0, L_Q = 131072, L_WM = L_Q, L_P = L_Q + 2048, L_FIN = L_Q + 16384;
constexpr u32 L_XD = 0  , L_QPART = 4096  , L_QV = 20480  , L_UV = 32768  , L_UK = 102400  , L_F1 = 65536  ,
              L_HD = 81920  , L_CP = 82176  , L_VD = 84224  , L_OPART = 86016  , L_STG = 102400  , L_OP2 = L_F1;
constexpr int SPIN_MAX = 20000;
#define RSLEEP 2
#define CPFW 15
#define WDELAY 0

__device__ __forceinline__ f32x4 mfma(u32x4 a, u32x4 b, f32x4 c) {
  return __builtin_amdgcn_mfma_f32_16x16x32_bf16(__builtin_bit_cast(bf16x8, a), __builtin_bit_cast(bf16x8, b), c, 0, 0, 0); }
typedef __bf16 bf16x2 __attribute__((ext_vector_type(2)));
__device__ __forceinline__ u32 pkbf(float a, float b) { return __builtin_bit_cast(u32, __builtin_convertvector(f32x2{a, b}, bf16x2)); }
__device__ __forceinline__ float asf(u32 v) { return __builtin_bit_cast(float, v); }
__device__ __forceinline__ u32 asu(float v) { return __builtin_bit_cast(u32, v); }
#define GLD(dst, voff, sbase) asm volatile("global_load_dwordx4 %0, %1, %2" : "=v"(dst) : "v"(voff), "s"(sbase))
#define GLDC(dst, voff, sbase) asm volatile("global_load_dwordx4 %0, %1, %2 sc1" : "=v"(dst) : "v"(voff), "s"(sbase))
#define GSTC(voff, data, sbase) asm volatile("global_store_dwordx4 %0, %1, %2 sc1\n\ts_nop 2" :: "v"(voff), "v"(data), "s"(sbase) : "memory")
#define GLDS(voff, sbase, ldsaddr) asm volatile("s_mov_b32 m0, %2\n\ts_nop 0\n\tglobal_load_lds_dwordx4 %0, %1" :: "v"(voff), "s"(sbase), "s"(ldsaddr) : "memory")
#define WAITV(n) asm volatile("s_waitcnt vmcnt(%0)" :: "n"(n) : "memory")
#define TIE(r) asm volatile("" : "+v"(r))
#define PIN(x) asm volatile("" : "+v"(x))
#define SB() __builtin_amdgcn_sched_barrier(0)
#define WAIT4(n, a, b, c, d) do { asm volatile("s_waitcnt vmcnt(%4)" : "+v"(a), "+v"(b), "+v"(c), "+v"(d) : "n"(n) : "memory"); } while (0)
#define WAIT2(n, a, b) do { asm volatile("s_waitcnt vmcnt(%2)" : "+v"(a), "+v"(b) : "n"(n) : "memory"); } while (0)
#define WAIT1(n, a) do { asm volatile("s_waitcnt vmcnt(%1)" : "+v"(a) : "n"(n) : "memory"); } while (0)
#define POLL1(a, o0, sb) asm volatile("global_load_dwordx4 %0, %1, %2 sc1\n\ts_waitcnt vmcnt(0)" : "=&v"(a) : "v"(o0), "s"(sb) : "memory")
#define POLL2(a, b, o0, o1, sb) asm volatile("global_load_dwordx4 %0, %2, %4 sc1\n\tglobal_load_dwordx4 %1, %3, %4 sc1\n\ts_waitcnt vmcnt(0)" : "=&v"(a), "=&v"(b) : "v"(o0), "v"(o1), "s"(sb) : "memory")
#define POLL4(a, b, c, d, o0, sb, i1, i2, i3) asm volatile("global_load_dwordx4 %0, %4, %8 sc1\n\tglobal_load_dwordx4 %1, %5, %8 sc1\n\tglobal_load_dwordx4 %2, %6, %8 sc1\n\tglobal_load_dwordx4 %3, %7, %8 sc1\n\ts_waitcnt vmcnt(0)" \
   : "=&v"(a), "=&v"(b), "=&v"(c), "=&v"(d) : "v"(o0), "v"((o0) + (i1)), "v"((o0) + (i2)), "v"((o0) + (i3)), "s"(sb) : "memory")
#define TAGOK(q) (((q)[1] == TL) & ((q)[3] == TH))
#define BAR() asm volatile("s_waitcnt lgkmcnt(0)\n\ts_barrier" ::: "memory")
template <int CTRL> __device__ __forceinline__ float dppf(float v) { return asf(__builtin_amdgcn_update_dpp(0, asu(v), CTRL, 0xf, 0xf, true)); }
__device__ __forceinline__ float sum16x(float v) { auto r = __builtin_amdgcn_permlane16_swap(asu(v), asu(v), false, false); return asf(r[0]) + asf(r[1]); }
__device__ __forceinline__ float sum32x(float v) { auto r = __builtin_amdgcn_permlane32_swap(asu(v), asu(v), false, false); return asf(r[0]) + asf(r[1]); }
__device__ __forceinline__ float max16x(float v) { auto r = __builtin_amdgcn_permlane16_swap(asu(v), asu(v), false, false); return fmaxf(asf(r[0]), asf(r[1])); }
__device__ __forceinline__ float max32x(float v) { auto r = __builtin_amdgcn_permlane32_swap(asu(v), asu(v), false, false); return fmaxf(asf(r[0]), asf(r[1])); }
__device__ __forceinline__ float max3(float a, float b, float c) { float d; asm("v_max3_f32 %0, %1, %2, %3" : "=v"(d) : "v"(a), "v"(b), "v"(c)); return d; }
__device__ __forceinline__ float max2(float a, float b) { float d; asm("v_max_f32 %0, %1, %2" : "=v"(d) : "v"(a), "v"(b)); return d; }
__device__ __forceinline__ u32 umax3(u32 a, u32 b, u32 c) { u32 d; asm("v_max3_u32 %0, %1, %2, %3" : "=v"(d) : "v"(a), "v"(b), "v"(c)); return d; }
__device__ __forceinline__ float wsum(float v) { v += dppf<0xB1>(v); v += dppf<0x4E>(v); v += dppf<0x141>(v); v += dppf<0x140>(v); v = sum16x(v); return sum32x(v); }
__device__ __forceinline__ float wmax(float v) { v = fmaxf(v, dppf<0xB1>(v)); v = fmaxf(v, dppf<0x4E>(v)); v = fmaxf(v, dppf<0x141>(v)); v = fmaxf(v, dppf<0x140>(v)); v = max16x(v); return max32x(v); }
__device__ __forceinline__ u32 swz(u32 r) { return ((r & 3) << 2) | (((r >> 3) & 1) << 1); }

__global__ __launch_bounds__(1024) void mla_ffn_fused(const char* __restrict__ x, const char* __restrict__ ckv, const char* __restrict__ wq,
    const char* __restrict__ wuk, const char* __restrict__ wuv, const char* __restrict__ wo, const char* __restrict__ w1,
    const char* __restrict__ w3, const char* __restrict__ w2, u16* __restrict__ out, char* __restrict__ ws) {
  __shared__ __attribute__((aligned(1024))) char smem[L_END];
  LDSP(char) sm = (LDSP(char))smem;
#define LD(T, off) (*(LDSP(T))(sm + (off)))
  const u32 tid = threadIdx.x, lane = tid & 63;
  const u32 w = __builtin_amdgcn_readfirstlane(tid >> 6);
  const u32 wg = blockIdx.x, xc = wg & 7, ii = wg >> 3;
  u32 dm = 0;
  if (w == CPFW) { u64 pc; asm volatile("s_getpc_b64 %0" : "=s"(pc)); pc &= ~127ull;
    const u32 co = min(ii * 3 + (lane & 3), 87u) * 128;
    asm volatile("global_load_dword %0, %1, %2" : "+v"(dm) : "v"(co), "s"(pc)); }
  const u32 r = lane & 15, g = lane >> 4;
  u64 E0, E; asm volatile("s_load_dwordx2 %0, %1, 0x0" : "=s"(E0) : "s"(ws));
  u32 TL, TH;
#define GET_TAGS() do { asm volatile("s_waitcnt lgkmcnt(0)\n\ts_mov_b64 %0, %1" : "=s"(E) : "s"(E0)); \
  u64 z = E + 0x9E3779B97F4A7C15ull; z = (z ^ (z >> 30)) * 0xBF58476D1CE4E5B9ull; z = (z ^ (z >> 27)) * 0x94D049BB133111EBull; z ^= z >> 31; \
  TL = (u32)z & 0xBFFFBFFFu; TH = (u32)(z >> 32) & 0xBFFFBFFFu; } while (0)

  if (ii < 16) {
    const u32 h = 4 * xc + (ii >> 2), j = ii & 3, wi = 4 * h + j;
    u32x4 mk;
    { const u32 hm = (r & 1) ? 0xFFFF0000u : 0x0000FFFFu, t = (r & 7) >> 1; mk = u32x4{t == 0 ? hm : 0u, t == 1 ? hm : 0u, t == 2 ? hm : 0u, t == 3 ? hm : 0u}; }
#define DB(xd) (u32x4{(xd) & mk[0], (xd) & mk[1], (xd) & mk[2], (xd) & mk[3]})
    const u32 bh = r >> 3, r7 = r & 7;
    u32x4 xr, ukr[2], wqr[2];
    if (w < 2) GLD(xr, w * 1024 + lane * 16, x);
    u32 owq = (128 * j + 8 * w + g) * 8192 + h * 256 + r * 16;
    GLD(wqr[0], owq, wq); GLD(wqr[1], owq + 32768, wq);
    if (w < 2) {
      WAIT1(2, xr);
      float f[8];
      #pragma unroll
      for (int e = 0; e < 4; ++e) { f[2 * e] = asf(xr[e] << 16); f[2 * e + 1] = asf(xr[e] & 0xFFFF0000u); }
      float s = 0; for (int e = 0; e < 8; ++e) s += f[e];
      const float mean = wsum(s) * (1.f / 512);
      float q = 0; for (int e = 0; e < 8; ++e) { f[e] -= mean; q += f[e] * f[e]; }
      const float rstd = 1.f / sqrtf(wsum(q) * (1.f / 512) + 1e-5f);
      #pragma unroll
      for (int e = 0; e < 8; ++e) { const u32 d = pkbf(f[e] * rstd, f[e] * rstd);
        LD(u32, L_XD + w * 2048 + 4 * ((lane >> 2) * 32 + (e & 3) * 8 + 2 * (lane & 3) + (e >> 2))) = d; }
    }
    GET_TAGS();
    BAR();
    GLD(ukr[0], owq, wuk); GLD(ukr[1], owq + 32768, wuk);
    {
      const u32x2 xd = LD(u32x2, L_XD + bh * 2048 + 4 * ((4 * j + (w >> 2)) * 32 + g * 8 + 2 * (w & 3)));
      f32x4 a = {0, 0, 0, 0};
      SB(); WAIT2(2, wqr[0], wqr[1]);
      asm volatile("" :: "v"(dm));
      a = mfma(wqr[0], DB(xd[0]), a); a = mfma(wqr[1], DB(xd[1]), a);
      #pragma unroll
      for (int e = 0; e < 4; ++e) LD(float, L_QPART + 4 * (w * 256 + bh * 128 + 32 * g + 8 * e + r7)) = a[e];
    }
    BAR();
    { float s = 0;
      #pragma unroll
      for (int k = 0; k < 4; ++k) s += LD(float, L_QPART + 4 * ((4 * g + k) * 256 + 16 * w + r));
      s = sum32x(sum16x(s));
      const float sn = dppf<0xB1>(s);
      u32x4 gr = {asu(s), TL, asu(sn), TH};
      if (g == 0 && !(r & 1)) GSTC(WS_QP + ((h * 4 + j) * 128 + 8 * w + (r >> 1)) * 16, gr, ws); }
    SB(); WAIT2(0, ukr[0], ukr[1]);
    #pragma unroll
    for (int k = 0; k < 2; ++k) { const u32 row = 8 * w + 4 * k + g; LD(u32x4, L_UK + row * 256 + ((r ^ swz(row & 15)) << 4)) = ukr[k]; }
    if (w < 2) { u32x4 q0, q1, q2, q3;
      const u32 o = WS_QP + (h * 4 * 128 + 64 * w + lane) * 16;
      _Pragma("nounroll") for (int it = 0; it < SPIN_MAX; ++it) { POLL4(q0, q1, q2, q3, o, ws, 2048, 4096, 6144);
        if (__all(TAGOK(q0) & TAGOK(q1) & TAGOK(q2) & TAGOK(q3))) break; }
      LD(u32, L_QV + 256 * w + 4 * lane) = pkbf((asf(q0[0]) + asf(q1[0])) + (asf(q2[0]) + asf(q3[0])), (asf(q0[2]) + asf(q1[2])) + (asf(q2[2]) + asf(q3[2]))); }
    BAR();
    if (w < 8) {
      u32x4 ua[4], qb[4];
      #pragma unroll
      for (int k = 0; k < 4; ++k) { ua[k] = LD(u32x4, L_UK + (16 * w + r) * 256 + (((4 * k + g) ^ swz(r)) << 4)); qb[k] = LD(u32x4, L_QV + (r & 1) * 256 + 64 * k + 16 * g); }
      SB();
      f32x4 a = {0, 0, 0, 0};
      #pragma unroll
      for (int k = 0; k < 4; ++k) a = mfma(ua[k], qb[k], a);
      const float sc = 0.08838834764831845f * 1.4426950408889634f;
      u32x4 gr = {pkbf(a[0] * sc, a[1] * sc), TL, pkbf(a[2] * sc, a[3] * sc), TH};
      if (r < 2) GSTC(WS_QA + ((r * 32 + h) * QA_STR + 32 * j + 4 * w + g) * 16, gr, ws);
    }
    BAR();
    if (WDELAY) __builtin_amdgcn_s_sleep(WDELAY);
    u32x4 f1r[2], uvr[2], w2r, wor[8];
    const u32 fb = min(wi >> 2, 21u), ks = wi & 3, ns = w & 3, kq = w >> 2;
    { const u32 col = min(64 * fb + 8 * r7, 1376u - 8);
      const u32 o = (SKIP && wi >= 88) ? 0u : (128 * ks + 8 * w + g) * 2752 + col * 2, st = (SKIP && wi >= 88) ? 0u : 4 * 2752;
      const char* wsel = bh ? w3 : w1;
      #pragma unroll
      for (int k = 0; k < 2; ++k) asm volatile("global_load_dwordx4 %0, %1, off" : "=v"(f1r[k]) : "v"(wsel + o + k * st)); }
    { const u32 o = (128 * j + 8 * w + g) * 8192 + h * 256 + r * 16;
      GLD(uvr[0], o, wuv); GLD(uvr[1], o + 32768, wuv); }
    asm volatile("s_barrier" ::: "memory");
    GLD(w2r, (SKIP && wi >= 86) ? 0u : (min(16 * wi, 1376u - 16) + 4 * kq + g) * 1024 + ns * 256 + r * 16, w2);
    u32 owo = (h * 128 + 32 * kq + g) * 1024 + ns * 256 + r * 16;
    #pragma unroll
    for (int k = 0; k < 2; ++k) GLD(wor[k], owo + ((k + 2 * j) & 7) * 4096, wo);
    { const u32x2 xd = LD(u32x2, L_XD + bh * 2048 + 4 * ((4 * ks + (w >> 2)) * 32 + g * 8 + 2 * (w & 3)));
      SB(); WAIT2(5, f1r[0], f1r[1]);
      f32x4 a = {0, 0, 0, 0};
      a = mfma(f1r[0], DB(xd[0]), a); a = mfma(f1r[1], DB(xd[1]), a);
      #pragma unroll
      for (int e = 0; e < 4; ++e) { const u32 row = 4 * g + e;
        LD(float, L_F1 + w * 1024 + 4 * (((bh * 64 + (row & 7) * 8 + r7) << 1) + (row >> 3))) = a[e]; }
      SB(); WAIT2(3, uvr[0], uvr[1]);
      #pragma unroll
      for (int k = 0; k < 2; ++k) { const u32 row = 8 * w + 4 * k + g; LD(u32x4, L_UV + row * 256 + ((r ^ swz(row & 15)) << 4)) = uvr[k]; }
    }
    BAR();
    if ((w >> 1) == 6) {
      f32x2 s = LD(f32x2, L_F1 + 8 * ((w & 1) * 64 + lane));
      #pragma unroll
      for (int k = 1; k < 16; ++k) s += LD(f32x2, L_F1 + k * 1024 + 8 * ((w & 1) * 64 + lane));
      u32x4 gr = {asu(s[0]), TL, asu(s[1]), TH};
      if (wi < 88 && 64 * fb + lane < 1376) GSTC(WS_FP + ((((w & 1) * 22 + fb) * 4 + ks) * 64 + lane) * 16, gr, ws);
    }
    asm volatile("s_barrier" ::: "memory");
    #pragma unroll
    for (int k = 2; k < 8; ++k) GLD(wor[k], owo + ((k + 2 * j) & 7) * 4096, wo);
    const u32 q4 = r >> 2, p4 = r & 3;
    u32x4 wof[8], uvf[4];
    { WAIT1(0, w2r); WAIT4(0, wor[0], wor[1], wor[2], wor[3]); WAIT4(0, wor[4], wor[5], wor[6], wor[7]);
      const u32 sb = w == 15 ? L_QPART : L_STG + 4096 * w;
      const u32 sw0 = sb + g * 256 + ((r ^ (g << 1)) << 4), sw1 = sb + (4 + g) * 256 + ((r ^ ((4 + g) << 1)) << 4);
      const u32 row = 4 * g + q4, sr = sb + row * 256 + (((p4 >> 1) ^ ((row & 7) << 1)) << 4) + 8 * (p4 & 1);
      #pragma unroll
      for (int hf = 0; hf < 2; ++hf) {
        LD(u32x4, sw0) = wor[4 * hf]; LD(u32x4, sw1) = wor[4 * hf + 1]; LD(u32x4, sw0 + 2048) = wor[4 * hf + 2]; LD(u32x4, sw1 + 2048) = wor[4 * hf + 3];
        asm volatile("" ::: "memory");
        #pragma unroll
        for (int i = 0; i < 8; ++i) { const u32x2 t = __builtin_bit_cast(u32x2, __builtin_amdgcn_ds_read_tr16_b64_v4i16((LDSP(s16x4))(sm + (sr ^ (i << 5)))));
          wof[i][2 * hf] = t[0]; wof[i][2 * hf + 1] = t[1]; }
        asm volatile("s_waitcnt lgkmcnt(0)" ::: "memory");
      }
    }
    if (w == 14) {
      float h0 = 0.f, h1 = 0.f;
      if (wi < 86) { u32x4 q0, q1; const u32 f0 = 16 * wi;
        const u32 o = WS_FP + (((f0 >> 6) * 4 + g) * 64 + (f0 & 63) + r) * 16;
        _Pragma("nounroll") for (int it = 0; it < SPIN_MAX; ++it) { POLL2(q0, q1, o, o + 22 * 4 * 64 * 16, ws);
          if (__all(TAGOK(q0) & TAGOK(q1))) break; __builtin_amdgcn_s_sleep(4); }
        const float g0 = sum32x(sum16x(asf(q0[0]))), u0 = sum32x(sum16x(asf(q0[2]))), g1 = sum32x(sum16x(asf(q1[0]))), u1 = sum32x(sum16x(asf(q1[2])));
        h0 = g0 / (1.f + __expf(-g0)) * u0; h1 = g1 / (1.f + __expf(-g1)) * u1; }
      if (lane < 16) { LD(u32, L_HD + 4 * lane) = pkbf(h0, h0); LD(u32, L_HD + 64 + 4 * lane) = pkbf(h1, h1); }
    }
    const u32 vi = (4 * (w & 1) + g - 2 * j) & 7, vw = (w - 4) & 7;
    u32 a_cp = L_CP + (r & 1) * 256 + 16 * g, a_vw = L_VD + (r & 1) * 256 + (vw >> 1) * 64 + (vi & 3) * 16 + (vi >> 2) * 8, a_vr = L_VD + (r & 1) * 256 + kq * 64 + g * 16,
        a_ow = L_OPART + (kq * 2 + (r & 1)) * 2112 + 4 * (128 * ns + 4 * g), a_or = L_OPART + ((tid >> 8) & 1) * 2112 + 8 * (tid & 255),
        a_op = WS_OP + (tid >> 3) * OP_STR + (wi * 8 + (tid & 7)) * 16,
        a_hd = L_HD + bh * 64 + 4 * (4 * kq + g), a_o2 = L_OP2 + 16 * (bh * 512 + 128 * ns + 32 * g + r7) + 4 * kq;
    PIN(a_cp); PIN(a_vw); PIN(a_vr); PIN(a_ow); PIN(a_or); PIN(a_op); PIN(a_hd); PIN(a_o2);
    if (w < 4) {
      const u32 cb = w & 1, ch = (w >> 1) & 1;
      u32x4 st, pt[8], al[8];
      const u32 os = WS_ST + ((cb * 64 + lane) * 32 + h) * 16;
      u32 op = WS_PT + ((cb * 16 + (h >> 1)) * 4 + j) * PT_STR + (32 * ch + g) * 512 + (r >> 2) * 128 + (h & 1) * 64 + (r & 3) * 16;
      u32 a_cw = L_CP + (ch * 2 + cb) * 256 + 2 * (32 * g + 16 * ((r >> 2) & 1) + (r & 3));
      PIN(op); PIN(a_cw);
      _Pragma("nounroll") for (int it = 0; it < SPIN_MAX; ++it) { bool ok = true; if (lane == ((5 * wi + 16 * w) & 63)) { POLL1(st, os, ws); ok = TAGOK(st); }
        if (__all(ok)) break; __builtin_amdgcn_s_sleep(RSLEEP); }
      _Pragma("nounroll") for (int it = 0; it < SPIN_MAX; ++it) { POLL1(st, os, ws); if (__all(TAGOK(st))) break; __builtin_amdgcn_s_sleep(2); }
#define PTLD() asm volatile("global_load_dwordx4 %0, %8, %10 sc1\n\tglobal_load_dwordx4 %1, %8, %10 offset:2048 sc1\n\t" \
      "global_load_dwordx4 %2, %9, %10 sc1\n\tglobal_load_dwordx4 %3, %9, %10 offset:2048 sc1\n\t" \
      "v_add_u32 %8, 0x2000, %8\n\tv_add_u32 %9, 0x2000, %9\n\t" \
      "global_load_dwordx4 %4, %8, %10 sc1\n\tglobal_load_dwordx4 %5, %8, %10 offset:2048 sc1\n\t" \
      "global_load_dwordx4 %6, %9, %10 sc1\n\tglobal_load_dwordx4 %7, %9, %10 offset:2048 sc1" \
      : "=&v"(pt[0]), "=&v"(pt[1]), "=&v"(pt[2]), "=&v"(pt[3]), "=&v"(pt[4]), "=&v"(pt[5]), "=&v"(pt[6]), "=&v"(pt[7]), "+v"(o0), "+v"(o1) : "s"(ws) : "memory")
#define PTWAIT() asm volatile("s_waitcnt vmcnt(0)" : "+v"(pt[0]), "+v"(pt[1]), "+v"(pt[2]), "+v"(pt[3]), "+v"(pt[4]), "+v"(pt[5]), "+v"(pt[6]), "+v"(pt[7]) :: "memory")
      { u32 o0 = op, o1 = op + 4096; PTLD(); }
      const float M = wmax(asf(st[0]));
      const u32 ab = pkbf(__builtin_amdgcn_exp2f(asf(st[0]) - M), 0.f) & 0xFFFFu;
      float Linv = 1.f / wsum(asf(ab << 16) * asf(st[2]));
      const u32 adup = ab | (ab << 16);
      #pragma unroll
      for (int k = 0; k < 8; ++k) { const u32 av = __builtin_amdgcn_ds_bpermute(4 * (32 * ch + 4 * k + g), adup); al[k] = DB(av); PIN(al[k]); }
      PIN(Linv);
      _Pragma("nounroll") for (int it = 0; it < SPIN_MAX; ++it) {
        PTWAIT();
        u32 mx = umax3(pt[0][1], pt[0][3], 0);
        #pragma unroll
        for (int k = 1; k < 8; ++k) mx = umax3(pt[k][1], pt[k][3], mx);
        if (__all(mx != 0xFFFFFFFFu)) break;
        u32 o0 = op, o1 = op + 4096; PTLD();
      }
      PTWAIT();
      f32x4 a0 = {0, 0, 0, 0}, a1 = {0, 0, 0, 0};
      #pragma unroll
      for (int k = 0; k < 8; k += 2) { a0 = mfma(pt[k], al[k], a0); a1 = mfma(pt[k + 1], al[k + 1], a1); }
      a0 = (a0 + a1) * Linv;
      const u32 p01 = pkbf(a0[0], a0[1]), p23 = pkbf(a0[2], a0[3]);
      if (r < 8) { LD(u16, a_cw) = (u16)p01; LD(u16, a_cw + 8) = (u16)(p01 >> 16); LD(u16, a_cw + 16) = (u16)p23; LD(u16, a_cw + 24) = (u16)(p23 >> 16); }
    } else if (w < 12) {
      const u32 chk = 2 * vw + (p4 >> 1), k0 = 8 * g + q4, ua = L_UV + k0 * 256 + ((chk ^ swz(k0 & 15)) << 4) + 8 * (p4 & 1);
      #pragma unroll
      for (int t = 0; t < 4; ++t) {
        const u32x2 b0 = __builtin_bit_cast(u32x2, __builtin_amdgcn_ds_read_tr16_b64_v4i16((LDSP(s16x4))(sm + ua + t * 8192)));
        const u32x2 b1 = __builtin_bit_cast(u32x2, __builtin_amdgcn_ds_read_tr16_b64_v4i16((LDSP(s16x4))(sm + ua + t * 8192 + 1024)));
        uvf[t] = u32x4{b0[0], b0[1], b1[0], b1[1]}; }
      PIN(uvf[0]); PIN(uvf[1]); PIN(uvf[2]); PIN(uvf[3]);
    }
    BAR();
    if (tid == 960) { const u32x4 gr = {0, TL, 0, TH}; GSTC(WS_FL + wi * 128, gr, ws); }
    if (w >= 4 && w < 12) {
      u32x4 c[8];
      #pragma unroll
      for (int t = 0; t < 8; ++t) c[t] = LD(u32x4, a_cp + (t >> 2) * 512 + (t & 3) * 64);
      SB();
      f32x4 a = {0, 0, 0, 0}, a2 = {0, 0, 0, 0};
      #pragma unroll
      for (int t = 0; t < 4; ++t) { a = mfma(uvf[t], c[t], a); a2 = mfma(uvf[t], c[4 + t], a2); }
      a += a2;
      if (r < 2) LD(u32x2, a_vw) = u32x2{pkbf(a[0], a[1]), pkbf(a[2], a[3])};
    }
    { const u32 hd = LD(u32, a_hd);
      f32x4 a0 = {0, 0, 0, 0};
      if (wi < 86) a0 = mfma(w2r, DB(hd), a0);
      #pragma unroll
      for (int e = 0; e < 4; ++e) LD(float, a_o2 + 128 * e) = a0[e];
    }
    BAR();
    { const u32x4 v = LD(u32x4, a_vr);
      f32x4 o[8];
      #pragma unroll
      for (int i = 0; i < 8; ++i) o[i] = mfma(wof[i], v, f32x4{0, 0, 0, 0});
      if (r < 2) {
        #pragma unroll
        for (int i = 0; i < 8; ++i) LD(f32x4, a_ow + 64 * i) = o[i]; }
    }
    BAR();
    if (tid < 512) {
      const f32x4 p0 = LD(f32x4, L_OP2 + 32 * tid), p1 = LD(f32x4, L_OP2 + 32 * tid + 16);
      const f32x2 yff = {(p0[0] + p0[1]) + (p0[2] + p0[3]), (p1[0] + p1[1]) + (p1[2] + p1[3])};
      const f32x2 s = ((LD(f32x2, a_or) + LD(f32x2, a_or + 4224)) + (LD(f32x2, a_or + 8448) + LD(f32x2, a_or + 12672))) + yff;
      u32x4 gr = {asu(s[0]), TL, asu(s[1]), TH};
      GSTC(a_op, gr, ws); }
  } else {
    const u32 T = xc * 16 + (ii - 16), ba = T >> 6, kt = T & 63;
    const u32 fz = swz(r), k8 = w & 7, ht = w >> 3;
    if (ADELAY) __builtin_amdgcn_s_sleep(ADELAY);
    u32 pto = WS_PT + ((ba * 16 + (r >> 1)) * 4 + (w >> 2)) * PT_STR + kt * 512 + (w & 3) * 128 + (r & 1) * 64 + g * 16;
    const char* ws2 = ws + 32 * PT_STR;
    { u32x4 cr[8];
      const u32 o = (ba * 8192 + kt * 128 + 8 * w) * 1024 + lane * 16;
      #pragma unroll
      for (int k = 0; k < 8; ++k) GLD(cr[k], o + k * 1024, ckv);
      GET_TAGS();
      const u32 lo = L_CKV + 8 * w * 1024, lx = lane ^ ((w & 1) << 1);
      #pragma unroll
      for (int k = 0; k < 8; k += 4) {
        SB(); WAIT4(4 - k, cr[k], cr[k + 1], cr[k + 2], cr[k + 3]);
        #pragma unroll
        for (int e = 0; e < 4; ++e) LD(u32x4, lo + (k + e) * 1024 + ((lx ^ (e << 2)) << 4)) = cr[k + e]; }
      asm volatile("" :: "v"(dm));
    }
    BAR();
    u32x4 af[16];
    #pragma unroll
    for (int t = 0; t < 16; ++t) af[t] = LD(u32x4, L_CKV + (16 * k8 + r) * 1024 + (((4 * t + g) ^ fz) << 4));
    const u32 q4 = r >> 2, p4 = r & 3;
    u32 a_qw0 = L_Q + (2 * w) * 1024 + (((lane >> 1) ^ swz((2 * w) & 15)) << 4) + 8 * (lane & 1),
        a_qw1 = L_Q + (2 * w + 1) * 1024 + (((lane >> 1) ^ swz((2 * w + 1) & 15)) << 4) + 8 * (lane & 1);
    u32 a_bq[4], a_pr[4];
    #pragma unroll
    for (int c = 0; c < 4; ++c) { const u32 xc_ = ((4 * c + g) ^ fz) << 4; a_bq[c] = L_Q + (16 * ht + r) * 1024 + xc_; a_pr[c] = L_P + r * 256 + xc_; PIN(a_bq[c]); PIN(a_pr[c]); }
    u32 a_wm = L_WM + 32 * (16 * ht + r), a_wmw = a_wm + 4 * k8, a_pw = L_P + (16 * ht + r) * 256 + (((2 * k8 + (g >> 1)) ^ fz) << 4) + 8 * (g & 1);
    u32 a_t0 = L_CKV + (8 * g + q4) * 1024 + (((4 * w + (p4 >> 1)) ^ swz((8 * g + q4) & 15)) << 4) + 8 * (p4 & 1), a_t1 = a_t0 ^ 32, a_t2 = a_t0 + 65536, a_t3 = a_t1 + 65536;
    u32 a_st = WS_ST + ((ba * 64 + kt) * 32 + 16 * ht + r) * 16;
    PIN(a_qw0); PIN(a_qw1); PIN(a_wm); PIN(a_wmw); PIN(a_pw); PIN(a_t0); PIN(a_t1); PIN(a_t2); PIN(a_t3); PIN(a_st); PIN(pto);
    { u32x4 q[4];
      const u32 o = WS_QA + ((ba * 32 + 2 * w) * QA_STR + lane) * 16;
      if (w == 0) {
        const u32 orp = WS_QA + ((ba * 32 + (kt & 31)) * QA_STR + 32 * ((kt >> 5) + 2 * (xc & 1)) + 31) * 16;
        _Pragma("nounroll") for (int it = 0; it < SPIN_MAX; ++it) { bool ok = true; if (lane == 0) { POLL1(q[0], orp, ws); ok = TAGOK(q[0]); }
          if (__all(ok)) break; __builtin_amdgcn_s_sleep(RSLEEP); } }
      BAR();
      _Pragma("nounroll") for (int it = 0; it < SPIN_MAX; ++it) {
        POLL4(q[0], q[1], q[2], q[3], o, ws, 1024, QA_STR * 16, QA_STR * 16 + 1024);
        if (__all(TAGOK(q[0]) & TAGOK(q[1]) & TAGOK(q[2]) & TAGOK(q[3]))) break;
        __builtin_amdgcn_s_sleep(1);
      }
      { const u32x4 em = {0xFFFFFFFFu, 0xFFFFFFFFu, 0xFFFFFFFFu, 0xFFFFFFFFu}; GSTC(pto, em, ws); GSTC(pto, em, ws2); }
      LD(u32x2, a_qw0) = u32x2{q[0][0], q[0][2]}; LD(u32x2, a_qw0 + 512) = u32x2{q[1][0], q[1][2]};
      LD(u32x2, a_qw1) = u32x2{q[2][0], q[2][2]}; LD(u32x2, a_qw1 + 512) = u32x2{q[3][0], q[3][2]};
    }
    BAR();
    f32x4 s0 = {0, 0, 0, 0}, s1 = {0, 0, 0, 0};
    { u32x4 bq[8];
      #pragma unroll
      for (int t = 0; t < 8; ++t) bq[t] = LD(u32x4, a_bq[t & 3] + (t >> 2) * 256);
      SB();
      #pragma unroll
      for (int t = 0; t < 8; ++t) { if (t & 1) s1 = mfma(af[t], bq[t], s1); else s0 = mfma(af[t], bq[t], s0);
        bq[t] = LD(u32x4, a_bq[t & 3] + (2 + (t >> 2)) * 256); }
      BAR();
      #pragma unroll
      for (int t = 0; t < 8; ++t) { if (t & 1) s1 = mfma(af[8 + t], bq[t], s1); else s0 = mfma(af[8 + t], bq[t], s0); }
    }
    s0 += s1;
    float m = max2(max3(s0[0], s0[1], s0[2]), s0[3]);
    { auto x = __builtin_amdgcn_permlane16_swap(asu(m), asu(m), false, false); m = max2(asf(x[0]), asf(x[1]));
      auto y = __builtin_amdgcn_permlane32_swap(asu(m), asu(m), false, false); m = max2(asf(y[0]), asf(y[1])); }
    LD(float, a_wmw) = m;
    BAR();
    { const f32x4 m0 = LD(f32x4, a_wm), m1 = LD(f32x4, a_wm + 16);
      m = max3(max3(m0[0], m0[1], m0[2]), max3(m0[3], m1[0], m1[1]), max2(m1[2], m1[3])); }
    s0 -= m;
    #pragma unroll
    for (int e = 0; e < 4; ++e) s0[e] = __builtin_amdgcn_exp2f(s0[e]);
    LD(u32x2, a_pw) = u32x2{pkbf(s0[0], s0[1]), pkbf(s0[2], s0[3])};
    WAITV(0);
    BAR();
    { f32x4 o[2][2] = {{{0, 0, 0, 0}, {0, 0, 0, 0}}, {{0, 0, 0, 0}, {0, 0, 0, 0}}};
      u32x4 bp0[4], bp1[4]; s16x4 ta[4][2][2];
      #pragma unroll
      for (int t = 0; t < 4; ++t) { bp0[t] = LD(u32x4, a_pr[t]); bp1[t] = LD(u32x4, a_pr[t] + 4096); }
      #pragma unroll
      for (int t = 0; t < 4; ++t) {
        #pragma unroll
        for (int ct = 0; ct < 2; ++ct) { const u32 a = (t < 2 ? (ct ? a_t1 : a_t0) : (ct ? a_t3 : a_t2)) + (t & 1) * 32768;
          ta[t][ct][0] = __builtin_amdgcn_ds_read_tr16_b64_v4i16((LDSP(s16x4))(sm + a));
          ta[t][ct][1] = __builtin_amdgcn_ds_read_tr16_b64_v4i16((LDSP(s16x4))(sm + a + 4096)); }
      }
      SB();
      if (k8 == 0) {
        const u32x4 one = {0x3F803F80u, 0x3F803F80u, 0x3F803F80u, 0x3F803F80u};
        f32x4 la = {0, 0, 0, 0};
        if (ht == 0) {
          #pragma unroll
          for (int t = 0; t < 4; ++t) la = mfma(one, bp0[t], la);
        } else {
          #pragma unroll
          for (int t = 0; t < 4; ++t) la = mfma(one, bp1[t], la);
        }
        const u32x4 sg = {asu(m), TL, asu(la[0]), TH};
        if (lane < 16) GSTC(a_st, sg, ws);
      }
      SB();
      #pragma unroll
      for (int t = 0; t < 4; ++t) {
        #pragma unroll
        for (int ct = 0; ct < 2; ++ct) {
          const u32x2 b0 = __builtin_bit_cast(u32x2, ta[t][ct][0]), b1 = __builtin_bit_cast(u32x2, ta[t][ct][1]);
          const u32x4 a = {b0[0], b0[1], b1[0], b1[1]};
          o[ct][0] = mfma(a, bp0[t], o[ct][0]); o[ct][1] = mfma(a, bp1[t], o[ct][1]); }
      }
      #pragma unroll
      for (int hx = 0; hx < 2; ++hx) {
        u32x4 d = {pkbf(o[0][hx][0], o[0][hx][1]), pkbf(o[0][hx][2], o[0][hx][3]), pkbf(o[1][hx][0], o[1][hx][1]), pkbf(o[1][hx][2], o[1][hx][3])};
        if (hx == 0) GSTC(pto, d, ws); else GSTC(pto, d, ws2); }
    }
    if ((T & 1) == 0) {
      const u32 cg = (T >> 1) & 31;
      u32x4 q; const u32 o = WS_OP + (ba * 32 + cg) * OP_STR + tid * 16;
      u32 xv = 0; if (tid < 16) xv = *(const u16*)(x + ba * 1024 + (16 * cg + tid) * 2);
      if (w == 0) {
        const u32 orp = WS_FL + ((5 * T + 3) & 127) * 128;
        _Pragma("nounroll") for (int it = 0; it < SPIN_MAX; ++it) { bool ok = true; if (lane == 0) { POLL1(q, orp, ws); ok = TAGOK(q); }
          if (__all(ok)) break; __builtin_amdgcn_s_sleep(RSLEEP); } }
      BAR();
      _Pragma("nounroll") for (int it = 0; it < SPIN_MAX; ++it) { POLL1(q, o, ws); if (__all(TAGOK(q))) break; __builtin_amdgcn_s_sleep(2); }
      float a = asf(q[0]), c = asf(q[2]);
      a += dppf<0x128>(a); c += dppf<0x128>(c);
      a = sum32x(sum16x(a)); c = sum32x(sum16x(c));
      if (lane < 8) LD(f32x2, L_FIN + w * 64 + 8 * lane) = f32x2{a, c};
      BAR();
      if (tid < 16) { float s = asf(xv << 16);
        #pragma unroll
        for (int k = 0; k < 16; ++k) s += LD(float, L_FIN + k * 64 + 4 * tid);
        out[ba * 512 + 16 * cg + tid] = (u16)(pkbf(s, s) & 0xFFFFu); }
      if (T == 0 && tid == 0) *(u64*)ws = E + 1;
    }
  }
}

extern "C" void kernel_launch(void* const* d_in, const int* in_sizes, int n_in, void* d_out, int out_size, void* d_ws, size_t ws_size, hipStream_t stream) {
  mla_ffn_fused<<<256, 1024, 0, stream>>>((const char*)d_in[0], (const char*)d_in[1], (const char*)d_in[2], (const char*)d_in[3], (const char*)d_in[4],
      (const char*)d_in[5], (const char*)d_in[7], (const char*)d_in[8], (const char*)d_in[9], (u16*)d_out, (char*)d_ws);
}
```
